```python
import torch

def _softmax(x, axis=-1):
    return torch.softmax(x, dim=axis)
def _gelu(x):
    return torch.nn.functional.gelu(x, approximate="tanh")
def _silu(x):
    return torch.nn.functional.silu(x)
def _sigmoid(x):
    return torch.sigmoid(x)
def _layernorm(x, axis=-1, eps=1e-5):
    m = x.mean(dim=axis, keepdim=True)
    v = x.var(dim=axis, keepdim=True, unbiased=False)
    return (x - m) / torch.sqrt(v + eps)
def _scatter_add(tgt, idx, src, axis=0):
    return tgt.clone().index_add_(axis, idx, src)
def _segment_sum(src, seg, n):
    out = torch.zeros((n,) + src.shape[1:], dtype=src.dtype)
    return out.index_add_(0, seg, src)
def _pad(x, pw):
    flat = tuple(v for pair in reversed(pw) for v in pair)
    return torch.nn.functional.pad(x, flat)

kernel_name = 'sequential_preLN__linear_attn_block__swiglu_ffn__prompt'

def setup_inputs():

    _w = (lambda a: (lambda t: t.to(torch.bfloat16) if t.is_floating_point() else t)(torch.as_tensor(a)))
    def _A_setup():
        B, S, D = 1, 2048, 448
        return {
            "x": _w((lambda *s: torch.randn(*s, dtype=torch.bfloat16) * 0.05)(B, S, D)),
            "wqkv": _w((lambda *s: torch.randn(*s, dtype=torch.bfloat16) * 0.1)(D, 3 * D)),
            "wo": _w((lambda *s: torch.randn(*s, dtype=torch.bfloat16) * 0.05)(D, D)),
        }
    _A_in = _A_setup()

    def _B_setup():
        B, S, D, F = 1, 2048, 448, 1792
        return {
            "x": _w((lambda *s: torch.randn(*s, dtype=torch.bfloat16) * 0.05)(B, S, D)),
            "w_gate": _w((lambda *s: torch.randn(*s, dtype=torch.bfloat16) * 0.05)(D, F)),
            "w_up": _w((lambda *s: torch.randn(*s, dtype=torch.bfloat16) * 0.05)(D, F)),
            "w_down": _w((lambda *s: torch.randn(*s, dtype=torch.bfloat16) * 0.05)(F, D)),
        }
    _B_in = _B_setup()

    return {
        "A_x": _A_in["x"],
        "A_wqkv": _A_in["wqkv"],
        "A_wo": _A_in["wo"],
        "B_x": _B_in["x"],
        "B_w_gate": _B_in["w_gate"],
        "B_w_up": _B_in["w_up"],
        "B_w_down": _B_in["w_down"],
    }

def reference(A_x, A_wqkv, A_wo, B_x, B_w_gate, B_w_up, B_w_down):
    def _A_ref(x, wqkv, wo):
        B, S, D = x.shape
        H = 16; Dh = D // H
        qkv = (lambda x, *d: x.permute(*d))((x @ wqkv).reshape(B, S, 3, H, Dh), 2, 0, 3, 1, 4)
        q, k, v = qkv[0], qkv[1], qkv[2]
        phi = lambda t: torch.where(t > 0, t + 1, torch.exp(t)) + 1e-6
        pq, pk = phi(q), phi(k)
        kv = torch.einsum("bhsd,bhse->bhde", pk, v)
        z = torch.einsum("bhsd->bhd", pk)
        num = torch.einsum("bhqd,bhde->bhqe", pq, kv)
        den = torch.einsum("bhqd,bhd->bhq", pq, z)[..., None] + 1e-6
        o = num / den
        return (lambda x, *d: x.permute(*d))(o, 0, 2, 1, 3).reshape(B, S, D) @ wo

    def _B_ref(x, w_gate, w_up, w_down):
        g = _silu(x @ w_gate)
        u = x @ w_up
        return (g * u) @ w_down

    x = A_x
    h = x + _A_ref(_layernorm(x, axis=-1), A_wqkv, A_wo)
    h2 = h + _B_ref(_layernorm(h, axis=-1), B_w_gate, B_w_up, B_w_down)
    return h2
```

```cpp
#include <hip/hip_runtime.h>
#include <hip/hip_bf16.h>
#include <stdint.h>

constexpr int S = 2048, D = 448, F = 1792, H = 16, DH = 28;
typedef unsigned short u16;
__device__ __forceinline__ float bf2f(u16 v) { return __uint_as_float(((unsigned)v) << 16); }
__device__ __forceinline__ u16 f2bf(float f) { __hip_bfloat16 b = __float2bfloat16(f); return *(u16*)&b; }

__global__ void ln_k(const u16* x, const float* xf, float* out) {
  int row = blockIdx.x; int t = threadIdx.x;
  float v[7]; float s = 0;
  for (int i = 0; i < 7; i++) { int c = t + 64 * i; v[i] = x ? bf2f(x[row * D + c]) : xf[row * D + c]; s += v[i]; }
  for (int o = 32; o; o >>= 1) s += __shfl_xor(s, o);
  float m = s / D; float q = 0;
  for (int i = 0; i < 7; i++) { float d = v[i] - m; q += d * d; }
  for (int o = 32; o; o >>= 1) q += __shfl_xor(q, o);
  float r = rsqrtf(q / D + 1e-5f);
  for (int i = 0; i < 7; i++) { int c = t + 64 * i; out[row * D + c] = bf2f(f2bf((v[i] - m) * r)); }
}
__global__ void gemm_k(const float* A, const u16* W, float* C, int M, int N, int K) {
  int n = blockIdx.x * 64 + threadIdx.x; int m = blockIdx.y;
  if (n >= N) return;
  float acc = 0;
  for (int k = 0; k < K; k++) acc += A[m * K + k] * bf2f(W[k * N + n]);
  C[m * N + n] = acc;
}
__device__ __forceinline__ float phi(float t) { return (t > 0 ? t + 1.f : __expf(t)) + 1e-6f; }
__global__ void kv_k(const float* qkv, float* kv) {
  int h = blockIdx.x; int d = threadIdx.x / 29, e = threadIdx.x % 29;
  float acc = 0;
  for (int s = 0; s < S; s++) {
    float pk = phi(qkv[s * 3 * D + D + h * DH + d]);
    float v = e < 28 ? qkv[s * 3 * D + 2 * D + h * DH + e] : 1.f;
    acc += pk * v;
  }
  kv[(h * DH + d) * 29 + e] = acc;
}
__global__ void o_k(const float* qkv, const float* kv, float* o) {
  int s = blockIdx.x; int h = threadIdx.x / DH, e = threadIdx.x % DH;
  float num = 0, den = 0;
  for (int d = 0; d < DH; d++) { float pq = phi(qkv[s * 3 * D + h * DH + d]); num += pq * kv[(h * DH + d) * 29 + e]; den += pq * kv[(h * DH + d) * 29 + 28]; }
  o[s * D + threadIdx.x] = bf2f(f2bf(num / (den + 1e-6f)));
}
__global__ void add_k(const u16* x, const float* a, float* h) { int i = blockIdx.x * 256 + threadIdx.x; h[i] = bf2f(x[i]) + a[i]; }
__global__ void act_k(const float* g, const float* u, float* a) { int i = blockIdx.x * 256 + threadIdx.x; float gg = g[i]; a[i] = bf2f(f2bf(gg / (1.f + __expf(-gg)) * u[i])); }
__global__ void out_k(const float* h, const float* d, u16* out) { int i = blockIdx.x * 256 + threadIdx.x; out[i] = f2bf(h[i] + d[i]); }

extern "C" void kernel_launch(void* const* d_in, const int* in_sizes, int n_in, void* d_out, int out_size, void* d_ws, size_t ws_size, hipStream_t stream) {
  const u16* x = (const u16*)d_in[0]; const u16* wqkv = (const u16*)d_in[1]; const u16* wo = (const u16*)d_in[2];
  const u16* wg = (const u16*)d_in[4]; const u16* wu = (const u16*)d_in[5]; const u16* wd = (const u16*)d_in[6];
  float* ws = (float*)d_ws;
  float* ln = ws; float* qkv = ln + S * D; float* kv = qkv + S * 3 * D; float* o = kv + 16384; float* att = o + S * D; float* h = att + S * D;
  float* g = h + S * D; float* u = g + S * F; float* a = u + S * F; float* dn = a + S * F;
  ln_k<<<S, 64, 0, stream>>>(x, nullptr, ln);
  gemm_k<<<dim3(21, S), 64, 0, stream>>>(ln, wqkv, qkv, S, 3 * D, D);
  kv_k<<<H, 28 * 29, 0, stream>>>(qkv, kv);
  o_k<<<S, D, 0, stream>>>(qkv, kv, o);
  gemm_k<<<dim3(7, S), 64, 0, stream>>>(o, wo, att, S, D, D);
  add_k<<<S * D / 256, 256, 0, stream>>>(x, att, h);
  ln_k<<<S, 64, 0, stream>>>(nullptr, h, ln);
  gemm_k<<<dim3(28, S), 64, 0, stream>>>(ln, wg, g, S, F, D);
  gemm_k<<<dim3(28, S), 64, 0, stream>>>(ln, wu, u, S, F, D);
  act_k<<<S * F / 256, 256, 0, stream>>>(g, u, a);
  gemm_k<<<dim3(7, S), 64, 0, stream>>>(a, wd, dn, S, D, F);
  out_k<<<S * D / 256, 256, 0, stream>>>(h, dn, (u16*)d_out);
}
```

```cpp
#include <hip/hip_runtime.h>
#include <stdint.h>

namespace {
constexpr int S = 2048, D = 448, F = 1792, H = 16;
typedef unsigned v4u __attribute__((ext_vector_type(4)));
typedef unsigned v2u __attribute__((ext_vector_type(2)));
typedef float f4 __attribute__((ext_vector_type(4)));
typedef float f2 __attribute__((ext_vector_type(2)));
typedef float f16v __attribute__((ext_vector_type(16)));
typedef __bf16 bf2 __attribute__((ext_vector_type(2)));
typedef __bf16 bf8 __attribute__((ext_vector_type(8)));
typedef __amdgpu_buffer_rsrc_t rsrc_t;
#define LAS __attribute__((address_space(3)))
#define GAS __attribute__((address_space(1)))
#define DI __device__ __forceinline__

constexpr int OFF_WQKV = 16384;
constexpr int OFF_WO = OFF_WQKV + 1344 * 1024;
constexpr int OFF_WC = OFF_WO + 448 * 1024;
constexpr int OFF_WD = OFF_WC + 3136 * 1024;
constexpr int OFF_PART = OFF_WD + 1568 * 1024;
constexpr int OFF_KVF = OFF_PART + 1024 * 1024;
constexpr int OFF_PQ = OFF_KVF + 32 * 1024;
constexpr int OFF_HF = OFF_PQ + 2048 * 1024;
constexpr int OFF_ACT = OFF_HF + 1792 * 1024;
constexpr int WS_BYTES = OFF_ACT + 7168 * 1024;

DI rsrc_t mk(const void* p, int bytes) { return __builtin_amdgcn_make_buffer_rsrc((void*)p, 0, bytes, 0x00020000); }
DI v4u ld128(rsrc_t r, int off) { return __builtin_amdgcn_raw_buffer_load_b128(r, off, 0, 0); }
DI void st128(v4u v, rsrc_t r, int off) { __builtin_amdgcn_raw_buffer_store_b128(v, r, off, 0, 0); }
DI f16v mfma32(v4u a, v4u b, f16v c) { return __builtin_amdgcn_mfma_f32_32x32x16_bf16(__builtin_bit_cast(bf8, a), __builtin_bit_cast(bf8, b), c, 0, 0, 0); }
DI f4 mfma16(v4u a, v4u b, f4 c) { return __builtin_amdgcn_mfma_f32_16x16x32_bf16(__builtin_bit_cast(bf8, a), __builtin_bit_cast(bf8, b), c, 0, 0, 0); }
DI unsigned pk2(float lo, float hi) { f2 v = {lo, hi}; return __builtin_bit_cast(unsigned, __builtin_convertvector(v, bf2)); }
DI float dot2(unsigned a, unsigned b, float c) { return __builtin_amdgcn_fdot2_f32_bf16(__builtin_bit_cast(bf2, a), __builtin_bit_cast(bf2, b), c, false); }
DI float bflo(unsigned u) { return __uint_as_float(u << 16); }
DI float bfhi(unsigned u) { return __uint_as_float(u & 0xffff0000u); }
DI float phi(float t) { return (t > 0.f ? t + 1.f : __builtin_amdgcn_exp2f(t * 1.44269504089f)) + 1e-6f; }
constexpr unsigned ONES = 0x3f803f80u;
DI v4u lds_rd(const char* sm, int off) { return *(const v4u*)(sm + off); }
DI void lds_wr(char* sm, int off, v4u v) { *(v4u*)(sm + off) = v; }
DI v4u f4u(float a, float b, float c, float d) { v4u r = {__float_as_uint(a), __float_as_uint(b), __float_as_uint(c), __float_as_uint(d)}; return r; }

constexpr int U_QKV = 672, U_WO = 224, U_WC = 1568, U_WD = 784, U_ALL = U_QKV + U_WO + U_WC + U_WD;
DI void pretile_unit(int u, int l, const void* wqkv, const void* wo, const void* wg, const void* wu, const void* wd, rsrc_t ws) {
  unsigned v[8];
  int dE, dO;
  const int r = l & 31, hh = l >> 5;
  if (u < U_QKV) {
    const int m = (u / 224 + 1) % 3, rem = u % 224, hq = rem & 3, ko = rem >> 2;
    const int head = 4 * hq + (l >> 4), rp = l & 15;
    rsrc_t src = mk(wqkv, D * 3 * D * 2);
    const int base = rp < 14 ? ((8 * ko) * 1344 + m * 448 + 28 * head + 2 * rp) * 2 : 0x40000000;
#pragma unroll
    for (int j = 0; j < 8; j++) v[j] = __builtin_amdgcn_raw_buffer_load_b32(src, base + j * 2688, 0, 0);
    dE = OFF_WQKV + ((m * 16 + head) * 28 + (ko >> 1)) * 1024 + ((ko & 1) * 32 + rp) * 16; dO = dE + 256;
  } else if (u < U_QKV + U_WO) {
    const int uu = u - U_QKV, T = uu % 7, ks = uu / 7, h = ks >> 1, s = ks & 1;
    rsrc_t src = mk(wo, D * D * 2);
    const int base = ((28 * h + s + 8 * hh) * 448 + 64 * T + 2 * r) * 2;
#pragma unroll
    for (int j = 0; j < 8; j++) { int o = base + (8 * (j >> 2) + (j & 3)) * 2 * 896; if (j >= 6 && hh) o = 0x40000000; v[j] = __builtin_amdgcn_raw_buffer_load_b32(src, o, 0, 0); }
    dE = OFF_WO + ((2 * T) * 32 + ks) * 1024 + l * 16; dO = dE + 32 * 1024;
  } else if (u < U_QKV + U_WO + U_WC) {
    const int uu = u - U_QKV - U_WO, T = uu % 56, ks = uu / 56;
    const char* wsel = (const char*)(r < 16 ? wg : wu) + ((16 * ks + 8 * hh) * F + 32 * T + 2 * (r & 15)) * 2;
#pragma unroll
    for (int j = 0; j < 8; j++) v[j] = *(const unsigned*)(wsel + j * F * 2);
    dE = OFF_WC + ((2 * T) * 28 + ks) * 1024 + l * 16; dO = dE + 28 * 1024;
  } else {
    const int uu = u - U_QKV - U_WO - U_WC, T = uu % 7, ft = uu / 7;
    rsrc_t src = mk(wd, F * D * 2);
    const int base = ((32 * (ft >> 1) + (ft & 1) + 8 * hh) * 448 + 64 * T + 2 * r) * 2;
#pragma unroll
    for (int j = 0; j < 8; j++) v[j] = __builtin_amdgcn_raw_buffer_load_b32(src, base + (8 * (j >> 2) + (j & 3)) * 2 * 896, 0, 0);
    dE = OFF_WD + ((2 * T) * 112 + ft) * 1024 + l * 16; dO = dE + 112 * 1024;
  }
  v4u e, o;
#pragma unroll
  for (int i = 0; i < 4; i++) { e[i] = __builtin_amdgcn_perm(v[2 * i + 1], v[2 * i], 0x05040100u); o[i] = __builtin_amdgcn_perm(v[2 * i + 1], v[2 * i], 0x07060302u); }
  st128(e, ws, dE); st128(o, ws, dO);
}
__global__ __launch_bounds__(512) void k_pretile(const void* wqkv, const void* wo, const void* wg, const void* wu, const void* wd, char* wsp) {
  rsrc_t ws = mk(wsp, WS_BYTES);
  const int w = __builtin_amdgcn_readfirstlane(threadIdx.x >> 6), l = threadIdx.x & 63;
  const int gw = blockIdx.x * 8 + w;
  pretile_unit(gw, l, wqkv, wo, wg, wu, wd, ws);
  if (gw + 2048 < U_ALL) pretile_unit(gw + 2048, l, wqkv, wo, wg, wu, wd, ws);
}

constexpr int A_X = 0;
constexpr int A_EX = 86016;
constexpr int A_KVIN = 118784;
constexpr int A_STAT = 135168;
constexpr int A_FIN = 139264;
constexpr int A_CS = 140288;
constexpr int A_CSQ = 146432;
constexpr int A_LDS = 146560;
__global__ __launch_bounds__(512) void k_A(const char* x, char* wsp) {
  extern __shared__ __attribute__((aligned(16))) char sm[];
  rsrc_t ws = mk(wsp, WS_BYTES);
  const int b = blockIdx.x, t = (b & 7) * 2 + (b >> 7), h = (b >> 3) & 15;
  const int w = __builtin_amdgcn_readfirstlane(threadIdx.x >> 6), l = threadIdx.x & 63;
  const int r = l & 31, hh = l >> 5, tt4 = w & 3, g = w >> 2;
  {
    const int r8 = l >> 3, c = l & 7;
#pragma unroll
    for (int rg = 0; rg < 2; rg++) {
      const char* src = x + (size_t)(t * 128 + 16 * w + 8 * rg + r8) * 896 + ((c ^ (4 * rg + (r8 >> 1))) * 16);
#pragma unroll
      for (int ln = 0; ln < 7; ln++)
        __builtin_amdgcn_global_load_lds((const GAS void*)(src + ln * 128), (LAS void*)(sm + A_X + ((2 * w + rg) * 7 + ln) * 1024), 16, 0, 0);
    }
  }
  v4u wst[7], wq[4];
#pragma unroll
  for (int i = 0; i < 7; i++) wst[i] = ld128(ws, OFF_WQKV + (((1 + g) * 16 + h) * 28 + 7 * tt4 + i) * 1024 + l * 16);
#pragma unroll
  for (int i = 0; i < 4; i++) wq[i] = ld128(ws, w < 7 ? OFF_WQKV + (h * 28 + 4 * w + i) * 1024 + l * 16 : 0x40000000);
  asm volatile("s_waitcnt vmcnt(0)" ::: "memory");
  __syncthreads();
  v4u xf[14];
  {
    const int row = 32 * tt4 + r;
    const int rb = A_X + (row >> 3) * 7 * 1024 + (row & 7) * 128, sw = (row >> 1) & 7;
#pragma unroll
    for (int i = 0; i < 14; i++) { const int ks = 14 * g + i; xf[i] = lds_rd(sm, rb + (ks >> 2) * 1024 + (((2 * (ks & 3) + hh) ^ sw) * 16)); }
  }
  {
    float s = 0.f, ss = 0.f;
#pragma unroll
    for (int i = 0; i < 14; i++)
#pragma unroll
      for (int j = 0; j < 4; j++) { s = dot2(xf[i][j], ONES, s); ss = dot2(xf[i][j], xf[i][j], ss); }
    *(f2*)(sm + A_STAT + ((32 * tt4 + r) * 4 + g * 2 + hh) * 8) = f2{s, ss};
  }
  __syncthreads();
  {
    float cs = 0.f;
#pragma unroll
    for (int i = 0; i < 7; i++) {
      lds_wr(sm, A_X + ((1 + g) * 28 + 7 * tt4 + i) * 1024 + l * 16, wst[i]);
#pragma unroll
      for (int j = 0; j < 4; j++) cs = dot2(wst[i][j], ONES, cs);
    }
    *(float*)(sm + A_CS + (((1 + g) * 32 + r) * 16 + tt4 * 2 + hh) * 4) = cs;
    float cq = 0.f;
#pragma unroll
    for (int i = 0; i < 4; i++) {
      if (w < 7) lds_wr(sm, A_X + (4 * w + i) * 1024 + l * 16, wq[i]);
#pragma unroll
      for (int j = 0; j < 4; j++) cq = dot2(wq[i][j], ONES, cq);
    }
    *(float*)(sm + A_CS + (r * 16 + w * 2 + hh) * 4) = cq;
    if (threadIdx.x < 128) {
      f4 p0 = *(const f4*)(sm + A_STAT + threadIdx.x * 32), p1 = *(const f4*)(sm + A_STAT + threadIdx.x * 32 + 16);
      const float s = (p0[0] + p0[2]) + (p1[0] + p1[2]), ss = (p0[1] + p0[3]) + (p1[1] + p1[3]);
      const float mean = s * (1.f / D), var = fmaxf(ss * (1.f / D) - mean * mean, 0.f);
      const float rstd = __builtin_amdgcn_rsqf(var + 1e-5f);
      *(float*)(sm + A_FIN + threadIdx.x * 4) = rstd; *(float*)(sm + A_FIN + 512 + threadIdx.x * 4) = mean * rstd;
    }
  }
  __syncthreads();
  f16v ak = {}, av = {}, aq = {};
#pragma unroll
  for (int i = 0; i < 14; i++) {
    const int ks = 14 * g + i;
    ak = mfma32(xf[i], lds_rd(sm, A_X + (28 + ks) * 1024 + l * 16), ak);
    av = mfma32(xf[i], lds_rd(sm, A_X + (56 + ks) * 1024 + l * 16), av);
  }
  float csk, csv;
  { f4 a = *(const f4*)(sm + A_CS + (32 + r) * 64), c = *(const f4*)(sm + A_CS + (32 + r) * 64 + 16); csk = ((a[0] + a[1]) + (a[2] + a[3])) + ((c[0] + c[1]) + (c[2] + c[3])); }
  { f4 a = *(const f4*)(sm + A_CS + (64 + r) * 64), c = *(const f4*)(sm + A_CS + (64 + r) * 64 + 16); csv = ((a[0] + a[1]) + (a[2] + a[3])) + ((c[0] + c[1]) + (c[2] + c[3])); }
  if (threadIdx.x < 32) { float q = 0.f;
#pragma unroll
    for (int i = 0; i < 4; i++) { f4 a = *(const f4*)(sm + A_CS + r * 64 + i * 16); q += (a[0] + a[1]) + (a[2] + a[3]); }
    *(float*)(sm + A_CSQ + r * 4) = q; }
  {
    const int eo = A_EX + (w ^ 4) * 4096 + l * 16;
    if (g == 0) { lds_wr(sm, eo, f4u(ak[8], ak[9], ak[10], ak[11])); lds_wr(sm, eo + 1024, f4u(ak[12], ak[13], ak[14], ak[15])); lds_wr(sm, eo + 2048, f4u(av[8], av[9], av[10], av[11])); lds_wr(sm, eo + 3072, f4u(av[12], av[13], av[14], av[15])); }
    else { lds_wr(sm, eo, f4u(ak[0], ak[1], ak[2], ak[3])); lds_wr(sm, eo + 1024, f4u(ak[4], ak[5], ak[6], ak[7])); lds_wr(sm, eo + 2048, f4u(av[0], av[1], av[2], av[3])); lds_wr(sm, eo + 3072, f4u(av[4], av[5], av[6], av[7])); }
  }
  __syncthreads();
  {
    float kk[8], vv[8];
#pragma unroll
    for (int j = 0; j < 8; j++) { kk[j] = g ? ak[8 + j] : ak[j]; vv[j] = g ? av[8 + j] : av[j]; }
    const int eo = A_EX + w * 4096 + l * 16;
    const bool valid = (r & 15) < 14;
    v4u pkf, vf;
#pragma unroll
    for (int q = 0; q < 2; q++) {
      f4 pk_ = *(const f4*)(sm + eo + q * 1024), pv = *(const f4*)(sm + eo + 2048 + q * 1024);
      const int tk = 32 * tt4 + 16 * g + 8 * q + 4 * hh;
      f4 a = *(const f4*)(sm + A_FIN + tk * 4), bb = *(const f4*)(sm + A_FIN + 512 + tk * 4);
      float ko[4], vo[4];
#pragma unroll
      for (int i = 0; i < 4; i++) {
        const float kx = (kk[4 * q + i] + pk_[i]) * a[i] - bb[i] * csk, vx = (vv[4 * q + i] + pv[i]) * a[i] - bb[i] * csv;
        ko[i] = valid ? phi(kx) : 0.f; vo[i] = valid ? vx : (r == 14 ? 1.f : 0.f);
      }
      pkf[2 * q] = pk2(ko[0], ko[1]); pkf[2 * q + 1] = pk2(ko[2], ko[3]); vf[2 * q] = pk2(vo[0], vo[1]); vf[2 * q + 1] = pk2(vo[2], vo[3]);
    }
    lds_wr(sm, A_KVIN + (tt4 * 2 + g) * 1024 + l * 16, pkf);
    lds_wr(sm, A_KVIN + 8192 + (tt4 * 2 + g) * 1024 + l * 16, vf);
  }
  __syncthreads();
  if (w < 4) {
    const int dq = w >> 1, eq = w & 1, r16 = l & 15, g4 = l >> 4;
    f4 acc = {};
#pragma unroll
    for (int kk = 0; kk < 4; kk++) {
      const int o = (2 * kk + (g4 >> 1)) * 1024 + ((g4 & 1) * 32 + r16) * 16;
      acc = mfma16(lds_rd(sm, A_KVIN + o + dq * 256), lds_rd(sm, A_KVIN + 8192 + o + eq * 256), acc);
    }
    st128(f4u(acc[0], acc[1], acc[2], acc[3]), ws, OFF_PART + (h * 16 + t) * 4096 + dq * 2048 + ((g4 & 1) * 32 + 16 * eq + r16) * 32 + (g4 >> 1) * 16);
  }
#pragma unroll
  for (int i = 0; i < 14; i++) aq = mfma32(lds_rd(sm, A_X + (14 * g + i) * 1024 + l * 16), xf[i], aq);
  {
    const int eo = A_EX + (w ^ 4) * 4096 + l * 16;
    if (g == 0) { lds_wr(sm, eo, f4u(aq[8], aq[9], aq[10], aq[11])); lds_wr(sm, eo + 1024, f4u(aq[12], aq[13], aq[14], aq[15])); }
    else { lds_wr(sm, eo, f4u(aq[0], aq[1], aq[2], aq[3])); lds_wr(sm, eo + 1024, f4u(aq[4], aq[5], aq[6], aq[7])); }
  }
  __syncthreads();
  {
    const int eo = A_EX + w * 4096 + l * 16;
    const float a = *(const float*)(sm + A_FIN + (32 * tt4 + r) * 4), bb = *(const float*)(sm + A_FIN + 512 + (32 * tt4 + r) * 4);
    v4u pf;
#pragma unroll
    for (int q = 0; q < 2; q++) {
      f4 p = *(const f4*)(sm + eo + q * 1024), cq = *(const f4*)(sm + A_CSQ + (16 * g + 8 * q + 4 * hh) * 4);
      float o[4];
#pragma unroll
      for (int i = 0; i < 4; i++) o[i] = phi(((g ? aq[8 + 4 * q + i] : aq[4 * q + i]) + p[i]) * a - bb * cq[i]);
      pf[2 * q] = pk2(o[0], o[1]); pf[2 * q + 1] = pk2(o[2], o[3]);
    }
    st128(pf, ws, OFF_PQ + (((4 * t + tt4) * 16 + h) * 2 + g) * 1024 + l * 16);
  }
}

__global__ __launch_bounds__(64) void k_red(char* wsp) {
  rsrc_t ws = mk(wsp, WS_BYTES);
  const int h = blockIdx.x >> 4, j16 = blockIdx.x & 15, l = threadIdx.x, p4 = l >> 4, c = l & 15;
  f4 s = {};
#pragma unroll
  for (int i = 0; i < 4; i++) s += __builtin_bit_cast(f4, ld128(ws, OFF_PART + (h * 16 + 4 * i + p4) * 4096 + (16 * j16 + c) * 16));
#pragma unroll
  for (int i = 0; i < 4; i++) { s[i] += __shfl_xor(s[i], 16); s[i] += __shfl_xor(s[i], 32); }
  if (l < 16) { v2u o = {pk2(s[0], s[1]), pk2(s[2], s[3])}; __builtin_amdgcn_raw_buffer_store_b64(o, ws, OFF_KVF + h * 2048 + (16 * j16 + c) * 8, 0, 0); }
}

DI void red_write(char* sm, int w, int l, const f16v& a0, const f16v& a1) {
#pragma unroll
  for (int q = 0; q < 4; q++) {
    lds_wr(sm, ((w * 2 + 0) * 4 + q) * 1024 + l * 16, f4u(a0[4 * q], a0[4 * q + 1], a0[4 * q + 2], a0[4 * q + 3]));
    lds_wr(sm, ((w * 2 + 1) * 4 + q) * 1024 + l * 16, f4u(a1[4 * q], a1[4 * q + 1], a1[4 * q + 2], a1[4 * q + 3]));
  }
}
DI v4u red_final(const char* sm, int q, int l, v4u res) {
  f4 e = {}, o = {};
#pragma unroll
  for (int w = 0; w < 8; w++) { e += *(const f4*)(sm + ((w * 2 + 0) * 4 + q) * 1024 + l * 16); o += *(const f4*)(sm + ((w * 2 + 1) * 4 + q) * 1024 + l * 16); }
  v4u out;
#pragma unroll
  for (int i = 0; i < 4; i++) out[i] = pk2(e[i] + bflo(res[i]), o[i] + bfhi(res[i]));
  return out;
}
__global__ __launch_bounds__(512) void k_B(const char* x, char* wsp) {
  extern __shared__ __attribute__((aligned(16))) char sm[];
  rsrc_t ws = mk(wsp, WS_BYTES);
  const int b = blockIdx.x, t = (b & 7) * 2 + (b >> 7), m = (b >> 3) & 15;
  if (m >= 14) return;
  const int T = m >> 1, a = m & 1;
  const int w = __builtin_amdgcn_readfirstlane(threadIdx.x >> 6), l = threadIdx.x & 63, r = l & 31, hh = l >> 5;
  v4u kvf[4], pq[2][4], wo[2][4];
#pragma unroll
  for (int i = 0; i < 4; i++) {
    kvf[i] = ld128(ws, OFF_KVF + (4 * w + i) * 1024 + l * 16);
    pq[0][i] = ld128(ws, OFF_PQ + ((4 * t + a) * 32 + 4 * w + i) * 1024 + l * 16);
    pq[1][i] = ld128(ws, OFF_PQ + ((4 * t + a + 2) * 32 + 4 * w + i) * 1024 + l * 16);
    wo[0][i] = ld128(ws, OFF_WO + ((2 * T) * 32 + 4 * w + i) * 1024 + l * 16);
    wo[1][i] = ld128(ws, OFF_WO + ((2 * T + 1) * 32 + 4 * w + i) * 1024 + l * 16);
  }
  const int ftt = 4 * t + a + 2 * (w >> 2), q = w & 3;
  const v4u xres = *(const v4u*)(x + (size_t)(ftt * 32 + r) * 896 + (64 * T + 16 * q + 8 * hh) * 2);
#pragma unroll
  for (int p = 0; p < 2; p++) {
    f16v acc0 = {}, acc1 = {};
#pragma unroll
    for (int hd = 0; hd < 2; hd++) {
      f16v n = {};
      n = mfma32(kvf[2 * hd], pq[p][2 * hd], n); n = mfma32(kvf[2 * hd + 1], pq[p][2 * hd + 1], n);
      const float den = __shfl(n[6], r + 32);
      const float inv = __builtin_amdgcn_rcpf(den + 1e-6f);
      v4u o0, o1;
#pragma unroll
      for (int i = 0; i < 4; i++) { o0[i] = pk2(n[2 * i] * inv, n[2 * i + 1] * inv); o1[i] = pk2(n[8 + 2 * i] * inv, n[9 + 2 * i] * inv); }
      acc0 = mfma32(wo[0][2 * hd], o0, acc0); acc1 = mfma32(wo[1][2 * hd], o0, acc1);
      acc0 = mfma32(wo[0][2 * hd + 1], o1, acc0); acc1 = mfma32(wo[1][2 * hd + 1], o1, acc1);
    }
    red_write(sm + p * 65536, w, l, acc0, acc1);
  }
  __syncthreads();
  const v4u hf = red_final(sm + (w >> 2) * 65536, q, l, xres);
  st128(hf, ws, OFF_HF + (ftt * 28 + 4 * T + q) * 1024 + l * 16);
}
__global__ __launch_bounds__(512) void k_C(char* wsp) {
  extern __shared__ __attribute__((aligned(16))) char sm[];
  rsrc_t ws = mk(wsp, WS_BYTES);
  const int b = blockIdx.x, t = (b & 7) * 2 + (b >> 7), c = (b >> 3) & 15;
  if (c >= 14) return;
  const int w = __builtin_amdgcn_readfirstlane(threadIdx.x >> 6), l = threadIdx.x & 63, r = l & 31, hh = l >> 5;
  const int ft = 8 * c + w;
  if (w < 7) {
#pragma unroll
    for (int tt = 0; tt < 4; tt++) {
      v4u hv[4]; float s = 0.f, ss = 0.f;
#pragma unroll
      for (int i = 0; i < 4; i++) hv[i] = ld128(ws, OFF_HF + ((4 * t + tt) * 28 + 4 * w + i) * 1024 + l * 16);
#pragma unroll
      for (int i = 0; i < 4; i++) {
        lds_wr(sm, (tt * 28 + 4 * w + i) * 1024 + l * 16, hv[i]);
#pragma unroll
        for (int j = 0; j < 4; j++) { s = dot2(hv[i][j], ONES, s); ss = dot2(hv[i][j], hv[i][j], ss); }
      }
      *(f2*)(sm + 114688 + ((w * 2 + hh) * 128 + tt * 32 + r) * 8) = f2{s, ss};
    }
  }
  v4u wc[28];
#pragma unroll
  for (int i = 0; i < 28; i++) wc[i] = ld128(ws, OFF_WC + (ft * 28 + i) * 1024 + l * 16);
  f16v cs = {};
  const v4u ones = {ONES, ONES, ONES, ONES};
#pragma unroll
  for (int i = 0; i < 28; i++) cs = mfma32(wc[i], ones, cs);
  __syncthreads();
#pragma unroll
  for (int tt = 0; tt < 4; tt++) {
    f16v acc = {};
#pragma unroll
    for (int i = 0; i < 28; i++) acc = mfma32(wc[i], lds_rd(sm, (tt * 28 + i) * 1024 + l * 16), acc);
    float s = 0.f, ss = 0.f;
#pragma unroll
    for (int i = 0; i < 14; i++) { f2 p = *(const f2*)(sm + 114688 + (i * 128 + tt * 32 + r) * 8); s += p[0]; ss += p[1]; }
    const float mean = s * (1.f / D), var = fmaxf(ss * (1.f / D) - mean * mean, 0.f);
    const float rstd = __builtin_amdgcn_rsqf(var + 1e-5f), mb = mean * rstd;
    float o[8];
#pragma unroll
    for (int j = 0; j < 8; j++) {
      const float gg = acc[j] * rstd - mb * cs[j], uu = acc[8 + j] * rstd - mb * cs[8 + j];
      o[j] = gg * uu * __builtin_amdgcn_rcpf(1.f + __builtin_amdgcn_exp2f(gg * -1.44269504089f));
    }
    v4u af = {pk2(o[0], o[1]), pk2(o[2], o[3]), pk2(o[4], o[5]), pk2(o[6], o[7])};
    st128(af, ws, OFF_ACT + ((4 * t + tt) * 112 + ft) * 1024 + l * 16);
  }
}
__global__ __launch_bounds__(512) void k_D(char* wsp, char* out) {
  extern __shared__ __attribute__((aligned(16))) char sm[];
  rsrc_t ws = mk(wsp, WS_BYTES);
  const int b = blockIdx.x, t = (b & 7) * 2 + (b >> 7), m = (b >> 3) & 15;
  if (m >= 14) return;
  const int T = m >> 1, a = m & 1;
  const int w = __builtin_amdgcn_readfirstlane(threadIdx.x >> 6), l = threadIdx.x & 63, r = l & 31, hh = l >> 5;
  const int ftt = 4 * t + a + 2 * (w >> 2), q = w & 3;
  const v4u hres = ld128(ws, OFF_HF + (ftt * 28 + 4 * T + q) * 1024 + l * 16);
  f16v acc[2][2] = {};
#pragma unroll
  for (int i = 0; i < 14; i++) {
    const v4u w0 = ld128(ws, OFF_WD + ((2 * T) * 112 + 14 * w + i) * 1024 + l * 16), w1 = ld128(ws, OFF_WD + ((2 * T + 1) * 112 + 14 * w + i) * 1024 + l * 16);
    const v4u a0 = ld128(ws, OFF_ACT + ((4 * t + a) * 112 + 14 * w + i) * 1024 + l * 16), a1 = ld128(ws, OFF_ACT + ((4 * t + a + 2) * 112 + 14 * w + i) * 1024 + l * 16);
    acc[0][0] = mfma32(w0, a0, acc[0][0]); acc[0][1] = mfma32(w1, a0, acc[0][1]);
    acc[1][0] = mfma32(w0, a1, acc[1][0]); acc[1][1] = mfma32(w1, a1, acc[1][1]);
  }
  red_write(sm, w, l, acc[0][0], acc[0][1]);
  red_write(sm + 65536, w, l, acc[1][0], acc[1][1]);
  __syncthreads();
  const v4u of = red_final(sm + (w >> 2) * 65536, q, l, hres);
  *(v4u*)(out + (size_t)(ftt * 32 + r) * 896 + (64 * T + 16 * q + 8 * hh) * 2) = of;
}
}

extern "C" void kernel_launch(void* const* d_in, const int* in_sizes, int n_in, void* d_out, int out_size, void* d_ws, size_t ws_size, hipStream_t stream) {
  const char* x = (const char*)d_in[0];
  char* ws = (char*)d_ws;
  (void)hipFuncSetAttribute((const void*)k_A, hipFuncAttributeMaxDynamicSharedMemorySize, A_LDS);
  (void)hipFuncSetAttribute((const void*)k_B, hipFuncAttributeMaxDynamicSharedMemorySize, 131072);
  (void)hipFuncSetAttribute((const void*)k_C, hipFuncAttributeMaxDynamicSharedMemorySize, 131072);
  (void)hipFuncSetAttribute((const void*)k_D, hipFuncAttributeMaxDynamicSharedMemorySize, 131072);
  k_pretile<<<256, 512, 0, stream>>>(d_in[1], d_in[2], d_in[4], d_in[5], d_in[6], ws);
  k_A<<<256, 512, A_LDS, stream>>>(x, ws);
  k_red<<<256, 64, 0, stream>>>(ws);
  k_B<<<256, 512, 131072, stream>>>(x, ws);
  k_C<<<256, 512, 131072, stream>>>(ws);
  k_D<<<256, 512, 131072, stream>>>(ws, (char*)d_out);
}
```

```cpp
#include <hip/hip_runtime.h>
#include <stdint.h>

constexpr int S = 2048, D = 448, F = 1792;
typedef unsigned v4u __attribute__((ext_vector_type(4)));
typedef unsigned v2u __attribute__((ext_vector_type(2)));
typedef float f4 __attribute__((ext_vector_type(4)));
typedef float f2 __attribute__((ext_vector_type(2)));
typedef float f16v __attribute__((ext_vector_type(16)));
typedef __bf16 bf2 __attribute__((ext_vector_type(2)));
typedef __bf16 bf8 __attribute__((ext_vector_type(8)));
typedef __amdgpu_buffer_rsrc_t rsrc_t;
#define LAS __attribute__((address_space(3)))
#define DI __device__ __forceinline__
#define SB() __builtin_amdgcn_sched_barrier(0)
#define MEMBAR() asm volatile("" ::: "memory")
#define BAR() do { SB(); asm volatile("s_waitcnt lgkmcnt(0)\n\ts_barrier" ::: "memory"); SB(); } while (0)

constexpr int OFF_EPOCH = 0, OFF_F1 = 4096, OFF_F2 = OFF_F1 + 8192;
constexpr int OFF_WQKV = 32768;
constexpr int OFF_WO = OFF_WQKV + 1344 * 1024;
constexpr int OFF_WC = OFF_WO + 448 * 1024;
constexpr int OFF_WD = OFF_WC + 112 * 29 * 1024;
constexpr int OFF_PART = OFF_WD + 1568 * 1024;
constexpr int OFF_KVF = OFF_PART + 1024 * 1024;
constexpr int OFF_PQ = OFF_KVF + 32 * 1024;
constexpr int OFF_HF = OFF_PQ + 2048 * 1024;
constexpr int OFF_ACT = OFF_HF + 1792 * 1024;
constexpr int WS_BYTES = OFF_ACT + 7168 * 1024;
constexpr int OOB = 0x40000000;
constexpr int SC1 = 16;
constexpr unsigned SENT = 0xFFFFFFFFu;
constexpr int SPIN_CAP = 1 << 16;

#define STAMP(i)

DI rsrc_t mk(const void* p, int bytes) { return __builtin_amdgcn_make_buffer_rsrc((void*)p, 0, bytes, 0x00020000); }
DI v4u ldf(rsrc_t r, int voff, int base, int i) { return __builtin_amdgcn_raw_buffer_load_b128(r, voff + (i & 3) * 1024, base + (i >> 2) * 4096, SC1); }
DI void stc(v4u v, rsrc_t r, int off) { __builtin_amdgcn_raw_buffer_store_b128(v, r, off, 0, SC1); }
DI v4u canon(v4u v) { v4u o; for (int i = 0; i < 4; i++) o[i] = min(v[i], SENT - 1u); return o; }
DI f16v mfma32(v4u a, v4u b, f16v c) { return __builtin_amdgcn_mfma_f32_32x32x16_bf16(__builtin_bit_cast(bf8, a), __builtin_bit_cast(bf8, b), c, 0, 0, 0); }
DI f4 mfma16(v4u a, v4u b, f4 c) { return __builtin_amdgcn_mfma_f32_16x16x32_bf16(__builtin_bit_cast(bf8, a), __builtin_bit_cast(bf8, b), c, 0, 0, 0); }
DI unsigned pk2(float lo, float hi) { f2 v = {lo, hi}; return __builtin_bit_cast(unsigned, __builtin_convertvector(v, bf2)); }
DI float dot2(unsigned a, unsigned b, float c) { return __builtin_amdgcn_fdot2_f32_bf16(__builtin_bit_cast(bf2, a), __builtin_bit_cast(bf2, b), c, false); }
DI float bflo(unsigned u) { return __uint_as_float(u << 16); }
DI float bfhi(unsigned u) { return __uint_as_float(u & 0xffff0000u); }
DI float phi(float t) { return (t > 0.f ? t + 1.f : __builtin_amdgcn_exp2f(t * 1.44269504089f)) + 1e-6f; }
constexpr unsigned ONES = 0x3f803f80u;
DI v4u lds_rd(const char* sm, int off) { return *(const v4u*)(sm + off); }
DI void lds_wr(char* sm, int off, v4u v) { *(v4u*)(sm + off) = v; }
DI v4u f4u(float a, float b, float c, float d) { v4u r = {__float_as_uint(a), __float_as_uint(b), __float_as_uint(c), __float_as_uint(d)}; return r; }
DI unsigned mx4(unsigned m, v4u v) { return max(max(max(m, v[0]), v[1]), max(v[2], v[3])); }
DI bool bad(unsigned m) { return __builtin_amdgcn_ballot_w64(m == SENT) != 0; }
DI float sum16(float v) { auto s = __builtin_amdgcn_permlane16_swap(__float_as_uint(v), __float_as_uint(v), false, false); return __uint_as_float(s[0]) + __uint_as_float(s[1]); }
DI float sum32(float v) { auto s = __builtin_amdgcn_permlane32_swap(__float_as_uint(v), __float_as_uint(v), false, false); return __uint_as_float(s[0]) + __uint_as_float(s[1]); }

struct Ptrs { const void *x, *wqkv, *wo, *wg, *wu, *wd; char* out; char* ws; };

struct Unit { unsigned v[8]; int dE, dO; };
constexpr int U_QKV = 672, U_WC = 1568, U_WO = 224, U_WD = 784, U_REST = U_WC + U_WO + U_WD;
DI void unit_loads(Unit& u, rsrc_t src, int voff, int voff2, int s1, int s4) {
#pragma unroll
  for (int j = 0; j < 8; j++) u.v[j] = __builtin_amdgcn_raw_buffer_load_b32(src, j >= 6 ? voff2 : voff, (j & 3) * s1 + (j >> 2) * s4, 0);
}
DI void unit_qkv(Unit& u, int m, int rem, bool ok, int l, const Ptrs& p) {
  const int hq = rem & 3, ko = rem >> 2;
  const int head = 4 * hq + (l >> 4), rp = l & 15;
  const int voff = (rp < 14 && ok) ? ((8 * ko) * 1344 + m * 448 + 28 * head + 2 * rp) * 2 : OOB;
  unit_loads(u, mk(p.wqkv, D * 3 * D * 2), voff, voff, 2688, 4 * 2688);
  u.dE = ok ? OFF_WQKV + (m * 16 + head) * 28 * 1024 + ko * 256 + rp * 16 : OOB; u.dO = u.dE + 14 * 1024;
}
DI void unit_rest(Unit& u, int n, int l, const Ptrs& p) {
  const int r = l & 31, hh = l >> 5;
  const void* src; int bytes, voff, voff2, s1;
  if (n < U_WC) {
    const int up = n & 1, T = (n >> 1) % 28, ks = (n >> 1) / 28;
    src = up ? p.wu : p.wg; bytes = D * F * 2; s1 = F * 2;
    voff = voff2 = ((16 * ks + 8 * hh) * F + 64 * T + 2 * r) * 2;
    u.dE = OFF_WC + ((4 * T + 2 * (r >> 4)) * 29 + ks) * 1024 + (hh * 32 + 16 * up + (r & 15)) * 16; u.dO = u.dE + 29 * 1024;
  } else if (n < U_WC + U_WO) {
    const int uu = n - U_WC, T = uu % 7, ks = uu / 7, h = ks >> 1, s = ks & 1;
    src = p.wo; bytes = D * D * 2; s1 = 1792;
    voff = ((28 * h + s + 8 * hh) * 448 + 64 * T + 2 * r) * 2; voff2 = hh ? OOB : voff;
    u.dE = OFF_WO + ((2 * T) * 32 + ks) * 1024 + l * 16; u.dO = u.dE + 32 * 1024;
  } else {
    const int uu = n - U_WC - U_WO, T = uu % 7, ft = uu / 7;
    src = p.wd; bytes = F * D * 2; s1 = 1792;
    voff = voff2 = n < U_REST ? ((32 * (ft >> 1) + (ft & 1) + 8 * hh) * 448 + 64 * T + 2 * r) * 2 : OOB;
    u.dE = n < U_REST ? OFF_WD + ((2 * T) * 112 + ft) * 1024 + l * 16 : OOB; u.dO = u.dE + 112 * 1024;
  }
  unit_loads(u, mk(src, bytes), voff, voff2, s1, 14336);
}
DI void unit_store(const Unit& u, rsrc_t ws) {
  v4u e, o;
#pragma unroll
  for (int i = 0; i < 4; i++) { e[i] = __builtin_amdgcn_perm(u.v[2 * i + 1], u.v[2 * i], 0x05040100u); o[i] = __builtin_amdgcn_perm(u.v[2 * i + 1], u.v[2 * i], 0x07060302u); }
  stc(e, ws, u.dE); stc(o, ws, u.dO);
}
DI bool flags_bad(v4u f, unsigned tag) { return __builtin_amdgcn_ballot_w64(((f[0] ^ tag) | (f[1] ^ tag) | (f[2] ^ tag) | (f[3] ^ tag)) != 0u) != 0; }
DI void wait_flags(rsrc_t ws, int off, int l, unsigned tag) {
  for (int spin = 0; spin < SPIN_CAP; spin++) {
    MEMBAR();
    if (!flags_bad(ldf(ws, l * 128, off, 0), tag)) break;
  }
}
template <int N> DI void poll_frags(v4u (&v)[N], rsrc_t ws, int voff, int base) {
  for (int spin = 0; spin < SPIN_CAP; spin++) {
    MEMBAR();
    unsigned m = 0;
#pragma unroll
    for (int i = 0; i < N; i++) v[i] = ldf(ws, voff, base, i);
#pragma unroll
    for (int i = 0; i < N; i++) m = mx4(m, v[i]);
    if (!bad(m)) break;
  }
}
DI void dma_rows(unsigned voff, const void* base, unsigned lds) {
  unsigned keep;
  asm volatile("s_mov_b32 %0, m0\n\ts_mov_b32 m0, %3\n\ts_nop 0\n\tglobal_load_lds_dwordx4 %1, %2\n\t"
               "s_add_u32 m0, m0, 896\n\ts_nop 0\n\tglobal_load_lds_dwordx4 %1, %2 offset:128\n\t"
               "s_add_u32 m0, m0, 896\n\ts_nop 0\n\tglobal_load_lds_dwordx4 %1, %2 offset:256\n\t"
               "s_add_u32 m0, m0, 896\n\ts_nop 0\n\tglobal_load_lds_dwordx4 %1, %2 offset:384\n\t"
               "s_add_u32 m0, m0, 896\n\ts_nop 0\n\tglobal_load_lds_dwordx4 %1, %2 offset:512\n\t"
               "s_add_u32 m0, m0, 896\n\ts_nop 0\n\tglobal_load_lds_dwordx4 %1, %2 offset:640\n\t"
               "s_add_u32 m0, m0, 896\n\ts_nop 0\n\tglobal_load_lds_dwordx4 %1, %2 offset:768\n\t"
               "s_mov_b32 m0, %0" : "=&s"(keep) : "v"(voff), "s"(base), "s"(lds) : "memory", "scc");
}
DI void dma_one(unsigned voff, const void* base, unsigned lds) {
  unsigned keep;
  asm volatile("s_mov_b32 %0, m0\n\ts_mov_b32 m0, %3\n\ts_nop 0\n\tglobal_load_lds_dwordx4 %1, %2\n\ts_mov_b32 m0, %0" : "=&s"(keep) : "v"(voff), "s"(base), "s"(lds) : "memory");
}
DI void red_write(char* sm, int w, int l, const f16v& a0, const f16v& a1) {
#pragma unroll
  for (int q = 0; q < 4; q++) {
    lds_wr(sm, ((w * 2 + 0) * 4 + q) * 1024 + l * 16, f4u(a0[4 * q], a0[4 * q + 1], a0[4 * q + 2], a0[4 * q + 3]));
    lds_wr(sm, ((w * 2 + 1) * 4 + q) * 1024 + l * 16, f4u(a1[4 * q], a1[4 * q + 1], a1[4 * q + 2], a1[4 * q + 3]));
  }
}
DI v4u red_final(const char* sm, int q, int l, v4u res) {
  f4 e = {bflo(res[0]), bflo(res[1]), bflo(res[2]), bflo(res[3])}, o = {bfhi(res[0]), bfhi(res[1]), bfhi(res[2]), bfhi(res[3])};
#pragma unroll
  for (int w = 0; w < 8; w++) { e += *(const f4*)(sm + ((w * 2 + 0) * 4 + q) * 1024 + l * 16); o += *(const f4*)(sm + ((w * 2 + 1) * 4 + q) * 1024 + l * 16); }
  v4u out;
#pragma unroll
  for (int i = 0; i < 4; i++) out[i] = pk2(e[i], o[i]);
  return out;
}

constexpr int L_X = 0;
constexpr int L_W = 16384;
constexpr int L_WQ = 73728;
constexpr int L_KVIN = 102400;
constexpr int L_WO = 0;
constexpr int L_R1 = 65792;
constexpr int L_HST = 131328;
constexpr int L_CSKV = 147456;
constexpr int L_CSQP = 147968;
constexpr int L_CSQ = 148480;
constexpr int L_ST = 148608;
constexpr int L_MISC = 149632;
constexpr int L_BYTES = 149760;

#define NPF1 4
#define NPF2 8
#define NPF3 16

__global__ __launch_bounds__(512, 2) void fused_block(Ptrs p) {
  __shared__ __attribute__((aligned(16))) char sm[L_BYTES];
  STAMP(0);
  const rsrc_t ws = mk(p.ws, WS_BYTES);
  const int b = blockIdx.x, xcd = b & 7, idx = b >> 3;
  const int tA = 8 * (xcd & 1) + (idx & 7), hA = 4 * (xcd >> 1) + (idx >> 3);
  const int tB = 2 * xcd + (idx >> 4), m = idx & 15, T = m >> 1, a = m & 1;
  const bool ffn = m < 14;
  const int ai = (idx >> 4) * 14 + m, tC = 4 * (xcd & 3) + (ai & 3), cC = 7 * (xcd >> 2) + (ai >> 2);
  const int jcs = xcd * 4 + (idx >> 4) * 2 + (m - 14);
  const int w = __builtin_amdgcn_readfirstlane(threadIdx.x >> 6), l = threadIdx.x & 63, l16 = l * 16;
  const int r = l & 31, c16 = l & 15, g4 = l >> 4;
  const v4u sent = {SENT, SENT, SENT, SENT};
  v4u xf[14];
  const int xrb = L_X + (2 * w + (c16 >> 3)) * 7168 + (c16 & 7) * 128, xsw = c16 >> 1;
  if (w == 0) {
    const unsigned ep = __builtin_amdgcn_raw_buffer_load_b32(ws, 0, OFF_EPOCH, SC1);
    Unit u0, u1, u2;
    unit_qkv(u0, b < 224 ? 1 : 2, b < 224 ? b : b - 224, true, l, p);
    unit_qkv(u1, b < 192 ? 2 : 0, b < 192 ? b + 32 : b - 192, true, l, p);
    unit_qkv(u2, 0, b < 160 ? b + 64 : 0, b < 160, l, p);
    SB();
#pragma unroll
    for (int i = 0; i < 4; i++) stc(sent, ws, l16 + OFF_PART + (hA * 16 + tA) * 4096 + i * 1024);
    SB();
    unit_store(u0, ws); unit_store(u1, ws); unit_store(u2, ws);
    const unsigned tag = __builtin_amdgcn_readfirstlane(ep) + 1u;
    asm volatile("s_waitcnt vmcnt(0)" ::: "memory");
    if (l == 0) {
      __builtin_amdgcn_raw_buffer_store_b32(tag, ws, 0, OFF_F1 + (b >> 2) * 128 + (b & 3) * 4, SC1);
      *(unsigned*)(sm + L_MISC) = tag;
    }
    STAMP(1);
    wait_flags(ws, OFF_F1, l, tag);
    if (b == 0 && l == 0) __builtin_amdgcn_raw_buffer_store_b32(tag, ws, 0, OFF_EPOCH, SC1);
  } else {
    const int r8 = l >> 3, c = l & 7;
    const char* xb = (const char*)p.x + (size_t)tA * 128 * 896;
    const unsigned v0 = r8 * 896 + ((c ^ (r8 >> 1)) * 16), v1 = (8 + r8) * 896 + ((c ^ (4 + (r8 >> 1))) * 16);
    const unsigned lb = (unsigned)(size_t)(LAS char*)sm + L_X;
    dma_rows(v0, xb + w * 14336, lb + w * 14336);
    dma_rows(v1, xb + w * 14336, lb + w * 14336 + 7168);
    dma_one(v0, xb + (w - 1) * 128, lb + (w - 1) * 1024);
    dma_one(v1, xb + (w - 1) * 128, lb + 7168 + (w - 1) * 1024);
    Unit u0, u1;
    unit_rest(u0, b * 10 + (w - 1), l, p);
    unit_rest(u1, w < 4 ? b * 10 + 6 + w : (w == 4 && b < 16 ? 2560 + b : U_REST), l, p);
    SB();
    asm volatile("s_waitcnt vmcnt(16)" ::: "memory");
    STAMP(1);
#pragma unroll
    for (int i = 0; i < 14; i++) xf[i] = lds_rd(sm, xrb + (i >> 1) * 1024 + (((4 * (i & 1) + g4) ^ xsw) * 16));
    unit_store(u0, ws); unit_store(u1, ws);
  }
  const int pq_off = OFF_PQ + ((4 * tA + (w >> 1)) * 16 + hA) * 2048 + g4 * 512 + (w & 1) * 256 + c16 * 16;
  stc(sent, ws, pq_off);
  stc(sent, ws, ffn ? l16 + OFF_HF + ((4 * tB + a + 2 * (w >> 2)) * 28 + 4 * T + (w & 3)) * 1024
                    : (w < 4 && jcs < 28 ? l16 + OFF_WC + ((4 * jcs + w) * 29 + 28) * 1024 : OOB));
  if (w == 7) stc(sent, ws, l < 8 ? l16 + OFF_KVF + hA * 2048 + tA * 128 : OOB);
  BAR();
  STAMP(2);
  const unsigned tag = __builtin_amdgcn_readfirstlane(*(const unsigned*)(sm + L_MISC));
  float st_a, st_b;
  f4 acc[4] = {};
  {
    const int cq = w & 3, u = w >> 2;
    const int wb = OFF_WQKV + ((1 + (cq >> 1)) * 16 + hA) * 28 * 1024 + (cq & 1) * 14 * 1024;
    v4u w1[4], w2[3], wq[4];
#pragma unroll
    for (int i = 0; i < 4; i++) w1[i] = ldf(ws, l16, wb + 4 * u * 1024, i);
#pragma unroll
    for (int i = 0; i < 3; i++) w2[i] = ldf(ws, l16, wb + (8 + 3 * u) * 1024, i);
    SB();
    if (w == 0) {
#pragma unroll
      for (int i = 0; i < 14; i++) xf[i] = lds_rd(sm, xrb + (i >> 1) * 1024 + (((4 * (i & 1) + g4) ^ xsw) * 16));
    }
    {
      float s = 0.f, ss = 0.f;
#pragma unroll
      for (int i = 0; i < 14; i++)
#pragma unroll
        for (int j = 0; j < 4; j++) { s = dot2(xf[i][j], ONES, s); ss = dot2(xf[i][j], xf[i][j], ss); }
      s = sum32(sum16(s)); ss = sum32(sum16(ss));
      const float mean = s * (1.f / D), var = fmaxf(ss * (1.f / D) - mean * mean, 0.f);
      st_a = __builtin_amdgcn_rsqf(var + 1e-5f); st_b = mean * st_a;
      if (l < 32) *(float*)(sm + L_ST + (w * 32 + l) * 4) = l < 16 ? st_a : st_b;
    }
    SB();
    float cs = 0.f;
#pragma unroll
    for (int i = 0; i < 4; i++) lds_wr(sm, L_W + (cq * 14 + 4 * u + i) * 1024 + l16, w1[i]);
    BAR();
    STAMP(3);
    if (threadIdx.x == 0) __builtin_amdgcn_raw_buffer_store_b32(tag, ws, 0, OFF_F2 + (b >> 2) * 128 + (b & 3) * 4, SC1);
    const int u4 = w >> 1, qk = u4 == 3 ? 11 : 4 * u4;
#pragma unroll
    for (int i = 0; i < 4; i++) wq[i] = ldf(ws, (i == 3 && u4 >= 2) ? OOB : l16, OFF_WQKV + (hA * 28 + (w & 1) * 14 + qk) * 1024, i);
#pragma unroll
    for (int i = 0; i < 3; i++) lds_wr(sm, L_W + (cq * 14 + 8 + 3 * u + i) * 1024 + l16, w2[i]);
    if (ffn) {
#pragma unroll
      for (int i = 0; i < 4; i++) stc(sent, ws, l16 + OFF_ACT + ((4 * tC + i) * 112 + 8 * cC + w) * 1024);
    }
    SB();
#pragma unroll
    for (int i = 0; i < 4; i++) {
#pragma unroll
      for (int q = 0; q < 4; q++) acc[q] = mfma16(xf[i], lds_rd(sm, L_W + (q * 14 + i) * 1024 + l16), acc[q]);
#pragma unroll
      for (int j = 0; j < 4; j++) cs = dot2(w1[i][j], ONES, cs);
      if (i < 3) {
#pragma unroll
        for (int j = 0; j < 4; j++) cs = dot2(w2[i][j], ONES, cs);
      }
    }
    cs = sum32(sum16(cs));
    if (l < 16) *(float*)(sm + L_CSKV + ((cq * 16 + l) * 2 + u) * 4) = cs;
    BAR();
    STAMP(4);
#pragma unroll
    for (int i = 4; i < 9; i++)
#pragma unroll
      for (int q = 0; q < 4; q++) acc[q] = mfma16(xf[i], lds_rd(sm, L_W + (q * 14 + i) * 1024 + l16), acc[q]);
    float cq_ = 0.f;
#pragma unroll
    for (int i = 0; i < 4; i++) {
      if (i < 3 || u4 < 2) lds_wr(sm, L_WQ + ((w & 1) * 14 + qk + i) * 1024 + l16, wq[i]);
#pragma unroll
      for (int j = 0; j < 4; j++) cq_ = dot2(wq[i][j], ONES, cq_);
    }
    cq_ = sum32(sum16(cq_));
    if (l < 16) *(float*)(sm + L_CSQP + (((w & 1) * 16 + l) * 4 + u4) * 4) = cq_;
  }
  v4u f2v;
  if (w == 7) f2v = ldf(ws, l * 128, OFF_F2, 0);
  SB();
#pragma unroll
  for (int i = 9; i < 14; i++)
#pragma unroll
    for (int q = 0; q < 4; q++) acc[q] = mfma16(xf[i], lds_rd(sm, L_W + (q * 14 + i) * 1024 + l16), acc[q]);
  {
    const f4 aa = *(const f4*)(sm + L_ST + w * 128 + g4 * 16), bb = *(const f4*)(sm + L_ST + w * 128 + 64 + g4 * 16);
    float csum[4];
#pragma unroll
    for (int q = 0; q < 4; q++) { const f2 t2 = *(const f2*)(sm + L_CSKV + (q * 16 + c16) * 8); csum[q] = t2[0] + t2[1]; }
    STAMP(5);
    const bool valid = c16 < 14;
    const unsigned vpad0 = c16 == 14 ? ONES : 0u;
#pragma unroll
    for (int q = 0; q < 4; q++) {
      float o[4];
#pragma unroll
      for (int i = 0; i < 4; i++) { o[i] = acc[q][i] * aa[i] - bb[i] * csum[q]; if (q < 2) o[i] = phi(o[i]); }
      const unsigned pad = q == 2 ? vpad0 : 0u;
      const v2u pr = {valid ? pk2(o[0], o[1]) : pad, valid ? pk2(o[2], o[3]) : pad};
      *(v2u*)(sm + L_KVIN + (q * 4 + (w >> 1)) * 1024 + l16 + (w & 1) * 8) = pr;
    }
  }
  if (w == 7) { if (flags_bad(f2v, tag)) wait_flags(ws, OFF_F2, l, tag); }
  asm volatile("s_waitcnt vmcnt(0)" ::: "memory");
  BAR();
  STAMP(6);
  if (w < 4) {
    f4 kv = {};
#pragma unroll
    for (int kk = 0; kk < 4; kk++) kv = mfma16(lds_rd(sm, L_KVIN + ((w >> 1) * 4 + kk) * 1024 + l16), lds_rd(sm, L_KVIN + ((2 + (w & 1)) * 4 + kk) * 1024 + l16), kv);
    stc(canon(f4u(kv[0], kv[1], kv[2], kv[3])), ws, l16 + OFF_PART + (hA * 16 + tA) * 4096 + w * 1024);
  }
  STAMP(7);
  const int vq = ffn ? l16 : OOB;
  v4u wos[8], wc[29];
#pragma unroll
  for (int i = 0; i < 8; i++) wos[i] = ldf(ws, vq, OFF_WO + (2 * T * 32 + 8 * w) * 1024, i);
  const int wc_off = OFF_WC + (8 * cC + w) * 29 * 1024;
#pragma unroll
  for (int i = 0; i < NPF1; i++) wc[i] = ldf(ws, vq, wc_off, i);
  SB();
  {
    f4 aq[2] = {};
#pragma unroll
    for (int i = 0; i < 14; i++) { aq[0] = mfma16(lds_rd(sm, L_WQ + i * 1024 + l16), xf[i], aq[0]); aq[1] = mfma16(lds_rd(sm, L_WQ + (14 + i) * 1024 + l16), xf[i], aq[1]); }
    v4u pf;
#pragma unroll
    for (int q = 0; q < 2; q++) {
      float o[4];
#pragma unroll
      for (int i = 0; i < 4; i++) { const f4 c = *(const f4*)(sm + L_CSQP + (16 * q + 4 * g4 + i) * 16); o[i] = phi(aq[q][i] * st_a - st_b * ((c[0] + c[1]) + (c[2] + c[3]))); }
      pf[2 * q] = pk2(o[0], o[1]); pf[2 * q + 1] = pk2(o[2], o[3]);
    }
    stc(canon(pf), ws, pq_off);
  }
  STAMP(8);
  if (w == 7) {
    v4u pv[4];
    const int vo = g4 * 4096 + (c16 >> 3) * 2048 + (c16 & 7) * 16, so = OFF_PART + hA * 65536 + ((tA >> 1) & 1) * 1024 + (tA >> 2) * 256 + (tA & 1) * 128;
    for (int spin = 0; spin < SPIN_CAP; spin++) {
      MEMBAR();
      unsigned mm = 0;
#pragma unroll
      for (int i = 0; i < 4; i++) pv[i] = ldf(ws, vo, so + i * 16384, 0);
#pragma unroll
      for (int i = 0; i < 4; i++) mm = mx4(mm, pv[i]);
      if (!bad(mm)) break;
    }
    f4 s = (__builtin_bit_cast(f4, pv[0]) + __builtin_bit_cast(f4, pv[1])) + (__builtin_bit_cast(f4, pv[2]) + __builtin_bit_cast(f4, pv[3]));
#pragma unroll
    for (int i = 0; i < 4; i++) s[i] = sum32(sum16(s[i]));
    const v2u o = {min(pk2(s[0], s[1]), SENT - 1u), min(pk2(s[2], s[3]), SENT - 1u)};
    __builtin_amdgcn_raw_buffer_store_b64(o, ws, l < 16 ? (c16 & 7) * 16 + (c16 >> 3) * 8 : OOB, OFF_KVF + hA * 2048 + tA * 128, SC1);
  }
  STAMP(9);
  if (!ffn) {
    if (jcs < 28) {
      const int ft = 4 * jcs + (w >> 1);
      v4u v[14]; float cs = 0.f;
#pragma unroll
      for (int i = 0; i < 14; i++) v[i] = ldf(ws, l16, OFF_WC + (ft * 29 + 14 * (w & 1)) * 1024, i);
#pragma unroll
      for (int i = 0; i < 14; i++)
#pragma unroll
        for (int j = 0; j < 4; j++) cs = dot2(v[i][j], ONES, cs);
      cs = sum32(cs);
      *(float*)(sm + L_HST + w * 256 + l * 4) = cs;
      BAR();
      if (!(w & 1)) {
        cs += *(const float*)(sm + L_HST + (w + 1) * 256 + l * 4);
        const unsigned hi = pk2(cs, cs), lo = pk2(cs - bflo(hi), 0.f);
        const v4u fr = {l < 32 ? min(hi, SENT - 1u) : 0u, l < 32 ? lo : 0u, 0u, 0u};
        stc(fr, ws, l16 + OFF_WC + (ft * 29 + 28) * 1024);
      }
    }
  } else {
#pragma unroll
    for (int i = 0; i < 8; i++) lds_wr(sm, L_WO + (w >> 2) * 128 + (8 * w + i) * 1024 + l16, wos[i]);
    const int c4 = w & 3, th = w >> 2;
    v2u xr[2];
#pragma unroll
    for (int ps = 0; ps < 2; ps++) xr[ps] = *(const v2u*)((const char*)p.x + (size_t)((4 * tB + a + 2 * ps) * 32 + 16 * th + c16) * 896 + (64 * T + 16 * c4 + 4 * g4) * 2);
#pragma unroll
    for (int i = NPF1; i < NPF2; i++) wc[i] = ldf(ws, l16, wc_off, i);
    BAR();
    STAMP(10);
    v4u wo[16], pq0[4], pq1[4], kvf[4];
#pragma unroll
    for (int i = 0; i < 16; i++) wo[i] = lds_rd(sm, L_WO + (c16 & 1) * 32896 + i * 2048 + g4 * 512 + (8 * c4 + (c16 >> 1)) * 16);
    poll_frags<4>(pq0, ws, l16, OFF_PQ + ((4 * tB + a) * 32 + 4 * w) * 1024);
    poll_frags<4>(pq1, ws, l16, OFF_PQ + ((4 * tB + a + 2) * 32 + 4 * w) * 1024);
    poll_frags<4>(kvf, ws, l16, OFF_KVF + 4 * w * 1024);
    STAMP(11);
#pragma unroll
    for (int ps = 0; ps < 2; ps++) {
#pragma unroll
      for (int hd = 0; hd < 2; hd++) {
        f16v n = {};
        n = mfma32(kvf[2 * hd], ps ? pq1[2 * hd] : pq0[2 * hd], n); n = mfma32(kvf[2 * hd + 1], ps ? pq1[2 * hd + 1] : pq0[2 * hd + 1], n);
        const auto sw = __builtin_amdgcn_permlane32_swap(__float_as_uint(n[6]), __float_as_uint(n[6]), false, false);
        const float inv = __builtin_amdgcn_rcpf(__uint_as_float(sw[1]) + 1e-6f);
        v4u o0, o1;
#pragma unroll
        for (int i = 0; i < 4; i++) { o0[i] = pk2(n[2 * i] * inv, n[2 * i + 1] * inv); o1[i] = pk2(n[8 + 2 * i] * inv, n[9 + 2 * i] * inv); }
        lds_wr(sm, L_R1 + ps * 32768 + (4 * w + 2 * hd) * 1024 + l16, o0);
        lds_wr(sm, L_R1 + ps * 32768 + (4 * w + 2 * hd + 1) * 1024 + l16, o1);
      }
      BAR();
      if (ps == 0) STAMP(12);
      f4 hacc = {bflo(xr[ps][0]), bfhi(xr[ps][0]), bflo(xr[ps][1]), bfhi(xr[ps][1])};
#pragma unroll
      for (int i = 0; i < 16; i++) hacc = mfma16(wo[i], lds_rd(sm, L_R1 + ps * 32768 + i * 2048 + g4 * 512 + th * 256 + c16 * 16), hacc);
      const unsigned p0 = min(pk2(hacc[0], hacc[1]), SENT - 1u), p1 = min(pk2(hacc[2], hacc[3]), SENT - 1u);
      const auto s0 = __builtin_amdgcn_permlane16_swap(p0, p0, false, false), s1 = __builtin_amdgcn_permlane16_swap(p1, p1, false, false);
      const v4u hf = {s0[0], s1[0], s0[1], s1[1]};
      stc(hf, ws, (g4 & 1) ? OOB : OFF_HF + ((4 * tB + a + 2 * ps) * 28 + 4 * T + c4) * 1024 + (g4 >> 1) * 512 + th * 256 + c16 * 16);
      STAMP(13 + ps);
      if (ps == 0) {
#pragma unroll
        for (int i = NPF2; i < NPF3; i++) wc[i] = ldf(ws, l16, wc_off, i);
        SB();
      }
    }
    v4u wd[2][14], av[14];
    const int wd_off = OFF_WD + ((2 * T) * 112 + 14 * w) * 1024;
    v4u hv[2][4];
    const int hoff = OFF_HF + (4 * tC * 28 + 4 * w) * 1024;
    if (w < 7) {
      poll_frags<4>(hv[0], ws, l16, hoff); poll_frags<4>(hv[1], ws, l16, hoff + 28 * 1024);
    }
    STAMP(15);
#pragma unroll
    for (int i = NPF3; i < 28; i++) wc[i] = ldf(ws, l16, wc_off, i);
    SB();
#pragma unroll
    for (int ps = 0; ps < 2; ps++) {
      const int hb = ps ? L_R1 : 0;
      if (w < 7) {
        if (ps == 1) { poll_frags<4>(hv[0], ws, l16, hoff + 56 * 1024); poll_frags<4>(hv[1], ws, l16, hoff + 84 * 1024); }
        float s[2] = {}, ss[2] = {};
#pragma unroll
        for (int k = 0; k < 2; k++)
#pragma unroll
          for (int i = 0; i < 4; i++) {
            lds_wr(sm, hb + (k * 28 + 4 * w + i) * 1024 + l16, hv[k][i]);
#pragma unroll
            for (int j = 0; j < 4; j++) { s[k] = dot2(hv[k][i][j], ONES, s[k]); ss[k] = dot2(hv[k][i][j], hv[k][i][j], ss[k]); }
          }
        const auto a1 = __builtin_amdgcn_permlane32_swap(__float_as_uint(s[0]), __float_as_uint(s[1]), false, false);
        const auto a2 = __builtin_amdgcn_permlane32_swap(__float_as_uint(ss[0]), __float_as_uint(ss[1]), false, false);
        *(f2*)(sm + L_HST + ((ps * 7 + w) * 64 + l) * 8) = f2{__uint_as_float(a1[0]) + __uint_as_float(a1[1]), __uint_as_float(a2[0]) + __uint_as_float(a2[1])};
      }
      BAR();
      if (ps == 0) STAMP(16);
      f16v ac[2] = {};
#pragma unroll
      for (int i = 0; i < 28; i++) {
        ac[0] = mfma32(wc[i], lds_rd(sm, hb + i * 1024 + l16), ac[0]);
        ac[1] = mfma32(wc[i], lds_rd(sm, hb + (28 + i) * 1024 + l16), ac[1]);
        if (ps == 0) {
          if (i == 12) { wc[28] = ldf(ws, l16, wc_off, 28); }
          if (i >= 14 && i < 26) { wd[i & 1][(i - 14) >> 1] = ldf(ws, l16, wd_off + (i & 1) * 112 * 1024, (i - 14) >> 1); }
          if ((i & 3) == 3) SB();
        } else {
          if (i < 16) wd[i & 1][6 + (i >> 1)] = ldf(ws, l16, wd_off + (i & 1) * 112 * 1024, 6 + (i >> 1));
          if (i >= 14) av[i - 14] = ldf(ws, l16, OFF_ACT + ((4 * tB + a) * 112 + 14 * w) * 1024, i - 14);
          if ((i & 3) == 3) SB();
        }
      }
      float c1[2], vv[2];
      {
        float s = 0.f, ss = 0.f;
#pragma unroll
        for (int i = 0; i < 7; i++) { const f2 q = *(const f2*)(sm + L_HST + ((ps * 7 + i) * 64 + l) * 8); s += q[0]; ss += q[1]; }
        const float nm = s * (-1.f / D), ve = fmaxf(ss * (1.f / D) - nm * nm, 0.f) + 1e-5f;
        const float cc = __builtin_amdgcn_rsqf(ve) * -1.44269504089f;
        const unsigned mh = pk2(nm, 0.f);
        const unsigned mm = pk2(nm, nm - bflo(mh));
        const auto s1 = __builtin_amdgcn_permlane32_swap(__float_as_uint(cc), __float_as_uint(cc), false, false);
        const auto s2 = __builtin_amdgcn_permlane32_swap(__float_as_uint(ve), __float_as_uint(ve), false, false);
        const auto s3 = __builtin_amdgcn_permlane32_swap(mm, mm, false, false);
        if (ps == 0) { if (bad(mx4(0u, wc[28]))) { v4u t1[1]; poll_frags<1>(t1, ws, l16, wc_off + 28 * 1024); wc[28] = t1[0]; } }
#pragma unroll
        for (int k = 0; k < 2; k++) {
          c1[k] = __uint_as_float(s1[k]); vv[k] = __uint_as_float(s2[k]);
          const v4u bx = {s3[k], s3[k] & 0xffffu, 0u, 0u};
          ac[k] = mfma32(wc[28], bx, ac[k]);
        }
      }
      STAMP(17 + 2 * ps);
#pragma unroll
      for (int k = 0; k < 2; k++) {
        float o[8];
#pragma unroll
        for (int j = 0; j < 8; j++) {
          const float e = __builtin_amdgcn_exp2f(ac[k][j] * c1[k]);
          o[j] = ac[k][j] * ac[k][8 + j] * __builtin_amdgcn_rcpf(__builtin_fmaf(e, vv[k], vv[k]));
        }
        const v4u af = {pk2(o[0], o[1]), pk2(o[2], o[3]), pk2(o[4], o[5]), pk2(o[6], o[7])};
        stc(canon(af), ws, l16 + OFF_ACT + ((4 * tC + 2 * ps + k) * 112 + 8 * cC + w) * 1024);
      }
      STAMP(18 + 2 * ps);
    }
    const int ftt = 4 * tB + a + 2 * (w >> 2);
    const v4u res = ldf(ws, l16, OFF_HF + (ftt * 28 + 4 * T + (w & 3)) * 1024, 0);
#pragma unroll
    for (int ps = 0; ps < 2; ps++) {
      const int aoff = OFF_ACT + ((4 * tB + a + 2 * ps) * 112 + 14 * w) * 1024;
      f16v acc0 = {}, acc1 = {};
#pragma unroll
      for (int i = 0; i < 14; i++) { acc0 = mfma32(wd[0][i], av[i], acc0); acc1 = mfma32(wd[1][i], av[i], acc1); }
      if (__builtin_amdgcn_ballot_w64(acc0[0] != acc0[0]) != 0) {
        poll_frags<14>(av, ws, l16, aoff);
        acc0 = f16v{}; acc1 = f16v{};
#pragma unroll
        for (int i = 0; i < 14; i++) { acc0 = mfma32(wd[0][i], av[i], acc0); acc1 = mfma32(wd[1][i], av[i], acc1); }
      }
      STAMP(21 + ps);
      if (ps == 0) {
        SB();
#pragma unroll
        for (int i = 0; i < 14; i++) av[i] = ldf(ws, l16, aoff + 2 * 112 * 1024, i);
        SB();
      }
      red_write(sm + (ps ? L_R1 : 0), w, l, acc0, acc1);
      BAR();
      if ((w >> 2) == ps) {
        const v4u of = red_final(sm + (ps ? L_R1 : 0), w & 3, l, res);
        *(v4u*)(p.out + (size_t)(ftt * 32 + r) * 896 + (64 * T + 16 * (w & 3) + 8 * (l >> 5)) * 2) = of;
      }
    }
  }
  STAMP(23);
}

extern "C" void kernel_launch(void* const* d_in, const int* in_sizes, int n_in, void* d_out, int out_size, void* d_ws, size_t ws_size, hipStream_t stream) {
  Ptrs p;
  p.x = d_in[0]; p.wqkv = d_in[1]; p.wo = d_in[2]; p.wg = d_in[4]; p.wu = d_in[5]; p.wd = d_in[6]; p.out = (char*)d_out; p.ws = (char*)d_ws;
  fused_block<<<256, 512, 0, stream>>>(p);
}
```

```cpp
#include <hip/hip_runtime.h>
#include <stdint.h>

constexpr int S = 2048, D = 448, F = 1792;
typedef unsigned v4u __attribute__((ext_vector_type(4)));
typedef unsigned v2u __attribute__((ext_vector_type(2)));
typedef float f4 __attribute__((ext_vector_type(4)));
typedef float f2 __attribute__((ext_vector_type(2)));
typedef float f16v __attribute__((ext_vector_type(16)));
typedef __bf16 bf2 __attribute__((ext_vector_type(2)));
typedef __bf16 bf8 __attribute__((ext_vector_type(8)));
typedef int srd_t __attribute__((ext_vector_type(4)));
typedef __amdgpu_buffer_rsrc_t rsrc_t;
#define DI __device__ __forceinline__
#define SB() __builtin_amdgcn_sched_barrier(0)
#define MEMBAR() asm volatile("" ::: "memory")
#define BAR() do { SB(); asm volatile("s_waitcnt lgkmcnt(0)\n\ts_barrier" ::: "memory"); SB(); } while (0)

constexpr int OFF_EPOCH = 0, OFF_FA = 32768;
constexpr int OFF_FS = 65536, OFF_FC = OFF_FS + 65536, OFF_FD = OFF_FC + 65536;
constexpr int OFF_WC = 262144;
constexpr int OFF_WD = OFF_WC + 112 * 29 * 1024;
constexpr int OFF_PART = OFF_WD + 1568 * 1024;
constexpr int OFF_KVF = OFF_PART + 2048 * 1024;
constexpr int OFF_PQ = OFF_KVF + 32 * 1024;
constexpr int OFF_HF = OFF_PQ + 2048 * 1024;
constexpr int OFF_HS = OFF_HF + 1792 * 1024;
constexpr int OFF_ACT = OFF_HS + 128 * 1024;
constexpr int WS_BYTES = OFF_ACT + 7168 * 1024;
constexpr int OOB = 0x40000000;
constexpr int SC1 = 16;
constexpr unsigned SENT = 0xFFFFFFFFu;
constexpr int SPIN_CAP = 1 << 16;

#define STAMP(i)
#define STAMPX(set, i)
#define DBG(i)
#define DBGA
#define DBGP

DI rsrc_t mk(const void* p, int bytes) { return __builtin_amdgcn_make_buffer_rsrc((void*)p, 0, bytes, 0x00020000); }
DI v4u ldf(rsrc_t r, int voff, int base, int i) { return __builtin_amdgcn_raw_buffer_load_b128(r, voff + (i & 3) * 1024, base + (i >> 2) * 4096, SC1); }
DI void stc(v4u v, rsrc_t r, int off) { __builtin_amdgcn_raw_buffer_store_b128(v, r, off, 0, SC1); }
DI v4u canon(v4u v) { v4u o; for (int i = 0; i < 4; i++) o[i] = min(v[i], SENT - 1u); return o; }
DI f16v mfma32(v4u a, v4u b, f16v c) { return __builtin_amdgcn_mfma_f32_32x32x16_bf16(__builtin_bit_cast(bf8, a), __builtin_bit_cast(bf8, b), c, 0, 0, 0); }
DI f4 mfma16(v4u a, v4u b, f4 c) { return __builtin_amdgcn_mfma_f32_16x16x32_bf16(__builtin_bit_cast(bf8, a), __builtin_bit_cast(bf8, b), c, 0, 0, 0); }
DI unsigned pk2(float lo, float hi) { f2 v = {lo, hi}; return __builtin_bit_cast(unsigned, __builtin_convertvector(v, bf2)); }
DI float dot2(unsigned a, unsigned b, float c) { return __builtin_amdgcn_fdot2_f32_bf16(__builtin_bit_cast(bf2, a), __builtin_bit_cast(bf2, b), c, false); }
DI float bflo(unsigned u) { return __uint_as_float(u << 16); }
DI float bfhi(unsigned u) { return __uint_as_float(u & 0xffff0000u); }
DI float phi(float t) { return (fmaxf(t, 0.f) + 1e-6f) + __builtin_amdgcn_fmed3f(__builtin_amdgcn_exp2f(t * 1.44269504089f), 0.f, 1.f); }
constexpr unsigned ONES = 0x3f803f80u;
DI v4u lds_rd(const char* sm, int off) { return *(const v4u*)(sm + off); }
DI void lds_wr(char* sm, int off, v4u v) { *(v4u*)(sm + off) = v; }
DI v4u f4u(float a, float b, float c, float d) { v4u r = {__float_as_uint(a), __float_as_uint(b), __float_as_uint(c), __float_as_uint(d)}; return r; }
DI unsigned mx4(unsigned m, v4u v) { return max(max(max(m, v[0]), v[1]), max(v[2], v[3])); }
DI bool bad(unsigned m) { return __builtin_amdgcn_ballot_w64(m == SENT) != 0; }
DI float sum16(float v) { auto s = __builtin_amdgcn_permlane16_swap(__float_as_uint(v), __float_as_uint(v), false, false); return __uint_as_float(s[0]) + __uint_as_float(s[1]); }
DI float sum32(float v) { auto s = __builtin_amdgcn_permlane32_swap(__float_as_uint(v), __float_as_uint(v), false, false); return __uint_as_float(s[0]) + __uint_as_float(s[1]); }
template <int CTRL> DI float dpp_add(float v) { return v + __builtin_bit_cast(float, __builtin_amdgcn_update_dpp(0, __builtin_bit_cast(int, v), CTRL, 0xf, 0xf, true)); }
DI float wave_sum(float v) { v = dpp_add<0xB1>(v); v = dpp_add<0x4E>(v); v = dpp_add<0x141>(v); v = dpp_add<0x140>(v); return sum32(sum16(v)); }
DI int fresh(int v) { asm volatile("" : "+v"(v)); return v; }
DI int sfresh(int v) { asm volatile("" : "+s"(v)); return v; }
DI int wrap(int v, int n) { return v >= n ? v - n : v; }
template <int I> struct IC { static constexpr int v = I; };
template <int I, int N, class Fn> DI void sfor(Fn&& f) { if constexpr (I < N) { f(IC<I>{}); sfor<I + 1, N>(f); } }

struct Ptrs { const void *x, *wqkv, *wo, *wg, *wu, *wd; char* out; char* ws; };

DI srd_t mks(const void* p, int bytes) { return srd_t{(int)(size_t)p, (int)((size_t)p >> 32) & 0xffff, bytes, 0x00020000}; }
DI v4u tag4(f4 v, unsigned tag) { v4u o; for (int i = 0; i < 4; i++) o[i] = __builtin_amdgcn_perm(__float_as_uint(v[i]), tag, 0x07060500u); return o; }
DI unsigned tagx(unsigned m, v4u v, unsigned tag) { return m | ((v[0] ^ tag) | (v[1] ^ tag)) | ((v[2] ^ tag) | (v[3] ^ tag)); }
DI unsigned lo2(unsigned a, unsigned b) { return __builtin_amdgcn_perm(b, a, 0x05040100u); }
DI unsigned hi2(unsigned a, unsigned b) { return __builtin_amdgcn_perm(b, a, 0x07060302u); }
DI void wait_flags(rsrc_t ws, int off, unsigned long long lines, int l, unsigned tag, int id DBGA) {
  const int vo = ((lines >> l) & 1) ? l * 128 : OOB;
#pragma nounroll
  for (int spin = 0; spin < SPIN_CAP; spin++) {
    MEMBAR();
    const v4u f = ldf(ws, vo, off, 0);
    if ((__builtin_amdgcn_ballot_w64(((f[0] ^ tag) | (f[1] ^ tag) | (f[2] ^ tag) | (f[3] ^ tag)) != 0u) & lines) == 0) break;
    DBG(id);
    __builtin_amdgcn_s_sleep(2);
  }
}
DI void set_flag(rsrc_t ws, int off, int b, bool on, int l, unsigned tag) {
  __builtin_amdgcn_raw_buffer_store_b32(tag, ws, (on && l < 8) ? l * 8192 : OOB, off + (b >> 2) * 128 + (b & 3) * 4, SC1);
}
DI void wait_words(unsigned v, unsigned tag, srd_t srd, int voff, int soff) {
  int cnt;
  asm volatile("s_mov_b32 %1, 0x10000\n"
    "1:\n\tv_cmp_ne_u32 vcc, %0, %2\n\ts_cbranch_vccz 2f\n\t"
    "buffer_load_dword %0, %3, %4, %5 offen sc1\n\ts_waitcnt vmcnt(0)\n\t"
    "s_sub_u32 %1, %1, 1\n\ts_cbranch_scc0 1b\n"
    "2:" : "+v"(v), "=&s"(cnt) : "v"(tag), "v"(voff), "s"(srd), "s"(soff) : "vcc", "scc", "memory");
}
template <int N> DI void poll_frags(v4u (&v)[N], rsrc_t ws, int voff, int base, int id DBGA) {
#pragma nounroll
  for (int spin = 0; spin < SPIN_CAP; spin++) {
    MEMBAR();
    const int lo = fresh(voff);
    unsigned m = 0;
#pragma unroll
    for (int i = 0; i < N; i++) v[i] = ldf(ws, lo, base, i);
#pragma unroll
    for (int i = 0; i < N; i++) m = mx4(m, v[i]);
    if (!bad(m)) break;
    DBG(id);
  }
}
template <class Fn> DI void poll8(v4u (&v)[8], rsrc_t ws, int voff, int s0, int s1, int id DBGA, Fn&& hook) {
  {
    const int lo = fresh(voff);
    unsigned m = 0;
#pragma unroll
    for (int i = 0; i < 8; i++) v[i] = ldf(ws, lo, i < 4 ? s0 : s1, i & 3);
    SB(); hook(); SB();
#pragma unroll
    for (int i = 0; i < 8; i++) m = mx4(m, v[i]);
    if (!bad(m)) return;
    DBG(id);
  }
#pragma nounroll
  for (int spin = 0; spin < SPIN_CAP; spin++) {
    MEMBAR();
    const int lo = fresh(voff);
    unsigned m = 0;
#pragma unroll
    for (int i = 0; i < 8; i++) v[i] = ldf(ws, lo, i < 4 ? s0 : s1, i & 3);
#pragma unroll
    for (int i = 0; i < 8; i++) m = mx4(m, v[i]);
    if (!bad(m)) break;
    DBG(id);
  }
}
typedef short s4v __attribute__((ext_vector_type(4)));
#define LAS __attribute__((address_space(3)))
DI v4u tr_frag(const char* sm, int off) {
  const v2u a = __builtin_bit_cast(v2u, __builtin_amdgcn_ds_read_tr16_b64_v4i16((LAS s4v*)((LAS char*)sm + off)));
  const v2u b = __builtin_bit_cast(v2u, __builtin_amdgcn_ds_read_tr16_b64_v4i16((LAS s4v*)((LAS char*)sm + off + 448)));
  return v4u{a[0], a[1], b[0], b[1]};
}
DI void dma_kv(int voff, srd_t srd, int soff, int lds) {
  unsigned keep;
  asm volatile("s_mov_b32 %0, m0\n\ts_mov_b32 exec_hi, 0x00ffffff\n\ts_mov_b32 m0, %4\n\ts_nop 0\n\t"
               "buffer_load_dwordx4 %1, %2, %3 offen offset:896 lds\n\tbuffer_load_dwordx4 %1, %2, %3 offen offset:1792 lds\n\t"
               "s_mov_b32 m0, %0\n\ts_mov_b32 exec_hi, -1" : "=&s"(keep) : "v"(voff), "s"(srd), "s"(soff), "s"(lds - 896) : "memory");
}
DI void dma_q(int voff, srd_t srd, int soff, int lds) {
  unsigned keep;
  asm volatile("s_mov_b32 %0, m0\n\ts_mov_b32 exec_hi, 0x00ffffff\n\ts_mov_b32 m0, %4\n\ts_nop 0\n\t"
               "buffer_load_dwordx4 %1, %2, %3 offen lds\n\t"
               "s_mov_b32 m0, %0\n\ts_mov_b32 exec_hi, -1" : "=&s"(keep) : "v"(voff), "s"(srd), "s"(soff), "s"(lds) : "memory");
}
DI void lds_arrive(char* sm, int off, int l) { if (l == 0) __hip_atomic_fetch_add((unsigned*)(sm + off), 1u, __ATOMIC_RELAXED, __HIP_MEMORY_SCOPE_WORKGROUP); }
DI void lds_wait(int addr, unsigned n) {
  unsigned t;
  asm volatile("1:\n\tds_read_b32 %0, %1\n\ts_waitcnt lgkmcnt(0)\n\tv_cmp_gt_u32 vcc, %2, %0\n\ts_cbranch_vccnz 1b" : "=&v"(t) : "v"(addr), "s"(n) : "vcc", "memory");
}
DI void dma_full(int voff, srd_t srd, int soff, int lds) {
  unsigned keep;
  asm volatile("s_mov_b32 %0, m0\n\ts_mov_b32 m0, %4\n\ts_nop 0\n\tbuffer_load_dwordx4 %1, %2, %3 offen lds\n\ts_mov_b32 m0, %0" : "=&s"(keep) : "v"(voff), "s"(srd), "s"(soff), "s"(lds) : "memory");
}
DI v2u tr_half(const char* sm, int off) { return __builtin_bit_cast(v2u, __builtin_amdgcn_ds_read_tr16_b64_v4i16((LAS s4v*)((LAS char*)sm + off))); }
DI void red_write(char* sm, int off, const f16v& a0, const f16v& a1) {
#pragma unroll
  for (int q = 0; q < 4; q++) {
    lds_wr(sm, off + q * 1024, f4u(a0[4 * q], a0[4 * q + 1], a0[4 * q + 2], a0[4 * q + 3]));
    lds_wr(sm, off + (4 + q) * 1024, f4u(a1[4 * q], a1[4 * q + 1], a1[4 * q + 2], a1[4 * q + 3]));
  }
}
DI v4u red_final(const char* sm, int off, v4u res) {
  v4u t[16];
#pragma unroll
  for (int i = 0; i < 16; i++) t[i] = lds_rd(sm, off + i * 4096);
  SB();
  f4 e = {bflo(res[0]), bflo(res[1]), bflo(res[2]), bflo(res[3])}, o = {bfhi(res[0]), bfhi(res[1]), bfhi(res[2]), bfhi(res[3])};
#pragma unroll
  for (int w = 0; w < 8; w++) { e += __builtin_bit_cast(f4, t[2 * w]); o += __builtin_bit_cast(f4, t[2 * w + 1]); }
  v4u out;
#pragma unroll
  for (int i = 0; i < 4; i++) out[i] = pk2(e[i], o[i]);
  return out;
}

constexpr int L_X = 0;
constexpr int L_KV = 57344;
constexpr int L_KVIN = 114688;
constexpr int L_WO = 57344;
constexpr int L_OB0 = 0, L_OB1 = 114688;
constexpr int L_H0 = 0;
constexpr int L_H1 = 65536;
constexpr int L_HST = 131328;
constexpr int L_XN = 149504;
constexpr int L_MISC = 157696;
constexpr int L_BYTES = 157824;

#define ABL 0
#define DLY_X 20
#define DLY_AV 10
#define ROTC 0
#define DLY_PQ 0
#define DLY_KF 0
#define DLY_F1 8
#define DLY_KV 16
#define NPF0 4
#define NPF1 10
#define NPF2 9
#define NPF3 15
#define NWD0 6

__global__ __launch_bounds__(512, 2) void fused_block(Ptrs p) {
  __shared__ __attribute__((aligned(16))) char sm[L_BYTES];
  STAMP(0);
  const rsrc_t ws = mk(p.ws, WS_BYTES);
  const int b = blockIdx.x, xcd = b & 7, idx = b >> 3;
  const int w = __builtin_amdgcn_readfirstlane(threadIdx.x >> 6), l = threadIdx.x & 63, l16 = l * 16;
  const int t64 = 8 * (xcd & 3) + (idx & 7), hp = 4 * (xcd >> 2) + (idx >> 3);
  const int rot = wrap((idx & 7) + 2 * (idx >> 3), 7) % 7;
  v4u xg[7];
  {
    const srd_t xs = mks(p.x, S * D * 2);
    const int xv = (l >> 3) * 896 + (l & 7) * 16, xso = (t64 * 64 + w * 8) * 896;
#pragma unroll
    for (int i = 0; i < 7; i++) asm volatile("buffer_load_dwordx4 %0, %1, %2, %3 offen" : "=v"(xg[i]) : "v"(xv), "s"(xs), "s"(xso + wrap(i + rot, 7) * 128) : "memory");
  }
  unsigned ep;
  {
    unsigned long long pc; int len;
    asm volatile("s_getpc_b64 %0\n.Lfb_pc:\n\ts_mov_b32 %1, .Lfb_end-.Lfb_pc" : "=s"(pc), "=s"(len));
    const srd_t es = mks(w < 4 ? (const void*)p.ws : (const void*)pc, w == 0 ? 64 : ((idx & 3) == w - 4 ? len : 0));
    asm volatile("buffer_load_dword %0, %1, %2, 0 offen sc1\n\ts_barrier" : "=v"(ep) : "v"(w < 4 ? 0 : l * 128 + (w - 4) * 8192), "s"(es) : "memory");
  }
  const srd_t wsrd = mks(p.wqkv, D * 3 * D * 2);
  const int l7 = (l * 37) >> 8, wv = l7 * 2688 + (l - 7 * l7) * 16 + hp * 112;
  const int lbase = (int)(size_t)(LAS char*)sm;
#define DMA_KV(c) dma_kv(wv, wsrd, (wrap((c) + rot, 7) * 8 + w) * 21504, lbase + L_KV + ((c) * 8 + w) * 1792)
#define DMA_Q(c) dma_q(wv, wsrd, (wrap((c) + rot, 7) * 8 + w) * 21504, lbase + L_X + ((c) * 8 + w) * 896)
  const int tB = 2 * xcd + (idx >> 4), m = idx & 15, T = m >> 1, a = m & 1;
  const bool ffn = m < 14;
  const int r = l & 31, c16 = l & 15, g4 = l >> 4, hh = l >> 5;
  const v4u sent = {SENT, SENT, SENT, SENT};
  const int tt = w & 1, hd = (w >> 1) & 1;
  const int rh = b >> 4, rj = b & 15;
  const int pq_off = OFF_PQ + ((2 * t64 + tt) * 16 + 2 * hp + hd) * 2048 + l16;
  const int part_off = OFF_PART + ((2 * hp + hd) * 32 + t64) * 4096 + (w & 1) * 2048 + l16;
  DMA_KV(0);
  unsigned ut[8];
  const int uks = 7 * ((b >> 1) & 3) + (w ? w - 1 : 0);
    {
      const srd_t us = mks((b & 1) ? p.wu : p.wg, D * F * 2);
      const int uv = (b < 224 && w) ? hh * 28672 + r * 4 : OOB, uo = uks * 57344 + idx * 128;
#pragma unroll
      for (int j = 0; j < 8; j++) asm volatile("buffer_load_dword %0, %1, %2, %3 offen" : "=v"(ut[j]) : "v"(uv), "s"(us), "s"(uo + j * 3584) : "memory");
    }
  SB();
  const int ai = (idx >> 4) * 14 + m, tC = 4 * (xcd & 3) + (ai & 3), cC = 7 * (xcd >> 2) + (ai >> 2);
  const int jcs = xcd * 4 + (idx >> 4) * 2 + (m - 14);
  *(v2u*)(sm + L_MISC + (threadIdx.x == 0 ? 8 : 16)) = v2u{0u, 0u};
  SB();
  asm volatile("s_waitcnt vmcnt(10)" : "+v"(ep), "+v"(xg[0]), "+v"(xg[1]), "+v"(xg[2]), "+v"(xg[3]), "+v"(xg[4]), "+v"(xg[5]), "+v"(xg[6]));
  STAMP(1);
  DMA_KV(1);
  {
    float s = 0.f, ss = 0.f;
#pragma unroll
    for (int i = 0; i < 7; i++)
#pragma unroll
      for (int j = 0; j < 4; j++) { s = dot2(xg[i][j], ONES, s); ss = dot2(xg[i][j], xg[i][j], ss); }
    SB(); DMA_KV(2); SB();
    s = dpp_add<0x141>(dpp_add<0x4E>(dpp_add<0xB1>(s))); ss = dpp_add<0x141>(dpp_add<0x4E>(dpp_add<0xB1>(ss)));
    const float mean = s * (1.f / D), var = fmaxf(ss * (1.f / D) - mean * mean, 0.f);
    const float ra = __builtin_amdgcn_rsqf(var + 1e-5f), rb = -mean * ra;
    const int o = L_X + w * 7168 + (l >> 3) * 128 + (((l & 7) ^ (4 * (w & 1) + (l >> 4))) * 16);
#pragma unroll
    for (int i = 0; i < 7; i++) {
      float f[8];
#pragma unroll
      for (int j = 0; j < 4; j++) { f[2 * j] = bflo(xg[i][j]); f[2 * j + 1] = bfhi(xg[i][j]); }
#pragma unroll
      for (int j = 0; j < 8; j++) { f[j] = __builtin_fmaf(f[j], ra, rb); asm volatile("" : "+v"(f[j])); }
      const v4u xn = {pk2(f[0], f[1]), pk2(f[2], f[3]), pk2(f[4], f[5]), pk2(f[6], f[7])};
      lds_wr(sm, o + i * 1024, xn);
      if (i == 3) { SB(); DMA_KV(3); SB(); }
    }
  }
  *(unsigned*)(sm + L_MISC + (threadIdx.x == 0 ? 0 : 16)) = ep + 1u;
  SB();
  DMA_KV(4);
  BAR();
  STAMP(2);
  v4u xf[28];
  {
    const int row = 32 * tt + r, xrb = L_X + (row >> 3) * 7168 + (row & 7) * 128, sw = (row >> 1) & 7;
    int xa[4];
#pragma unroll
    for (int i = 0; i < 4; i++) xa[i] = xrb + (((2 * i + hh) ^ sw) * 16);
#pragma unroll
    for (int i = 0; i < 28; i++) xf[i] = lds_rd(sm, xa[i & 3] + (i >> 2) * 1024);
  }
  const unsigned tag = *(const unsigned*)(sm + L_MISC);
  const int trl = (c16 >> 2) * 112 + hd * 56 + min(4 * (g4 & 1) + (c16 & 3), 6) * 8;
  f16v acc = {};
  const int ba = fresh(L_KV + (w >> 2) * 896 + hh * 1792 + trl), bb2 = fresh(L_KV + (w >> 2) * 896 + hh * 1792 + trl + 57344);
#define RD_KV(i) tr_frag(sm, (i) < 16 ? ba + (i) * 3584 : bb2 + ((i) - 16) * 3584)
  v4u bb[4];
  {
    SB();
    {
      asm volatile("s_waitcnt vmcnt(8)" : "+v"(ut[0]), "+v"(ut[1]), "+v"(ut[2]), "+v"(ut[3]), "+v"(ut[4]), "+v"(ut[5]), "+v"(ut[6]), "+v"(ut[7]));
      const int d0 = w ? (b < 224 ? OFF_WC + (116 * idx + uks) * 1024 + (g4 & 1) * 59392 + hh * 512 + (b & 1) * 256 + c16 * 16 : OOB) : (l < 8 ? l16 + OFF_KVF + rh * 2048 + rj * 128 : OOB);
      const int d1 = w ? d0 + 29696 : ((ffn && l < 32) ? l16 + OFF_HS + ((tB * 8 + T) * 2 + a) * 512 : OOB);
      const unsigned f_ = w ? 0u : SENT;
      stc(v4u{lo2(ut[0], ut[1]), lo2(ut[2], ut[3]), lo2(ut[4], ut[5]), lo2(ut[6], ut[7])} | f_, ws, d0);
      stc(v4u{hi2(ut[0], ut[1]), hi2(ut[2], ut[3]), hi2(ut[4], ut[5]), hi2(ut[6], ut[7])} | f_, ws, d1);
      SB(); DMA_KV(5); SB();
      stc(sent, ws, pq_off + (w >> 2) * 1024);
      stc(sent, ws, ffn ? l16 + OFF_HF + ((4 * tB + a + 2 * (w >> 2)) * 28 + 4 * T + (w & 3)) * 1024 : OOB);
      SB(); DMA_KV(6);
    }
    SB(); asm volatile("s_waitcnt vmcnt(10) lgkmcnt(0)\n\ts_barrier" ::: "memory"); SB();
#pragma unroll
    for (int i = 0; i < 3; i++) bb[i] = RD_KV(i);
    sfor<0, 13>([&](auto II) {
      constexpr int i = decltype(II)::v;
      if constexpr (i % 7 == 0) __builtin_amdgcn_s_setprio(3 - i / 7);
      if constexpr (i == 4 || i == 8 || i == 12) DMA_Q(i / 4 - 1);
      bb[(i + 3) & 3] = RD_KV(i + 3);
      acc = mfma32(xf[i], bb[i & 3], acc);
      SB();
    });
    asm volatile("s_waitcnt vmcnt(3) lgkmcnt(0)\n\ts_barrier" ::: "memory"); SB();
    STAMP(3);
  }
  v4u wc[29];
  const int wc_off = OFF_WC + (8 * cC + w) * 29 * 1024, rq = ROTC ? (ai & 3) : 0;
#define LD_WC(i) wc[i] = ldf(ws, lo, wc_off + ((((i) / 7) + rq) & 3) * 7168, (i) % 7)
  const srd_t os = mks(p.wo, ffn ? D * D * 2 : 0);
  const int ov = (l >> 3) * 896 + (((l & 7) ^ (2 * ((l >> 4) & 3))) * 16) + 128 * T, og = 14 * (2 * (idx >> 4) + a);
#define DMA_WO(g) do { const int gg_ = wrap((g) + og, 56); dma_full(ov, os, gg_ * 7168, lbase + L_WO + gg_ * 1024); } while (0)
  if (w < 4) {
    __builtin_amdgcn_s_setprio(3);
    sfor<13, 28>([&](auto II) {
      constexpr int i = decltype(II)::v;
      if constexpr (i == 21) { SB(); asm volatile("s_barrier" ::: "memory"); SB(); }
      if constexpr (i + 3 < 28) bb[(i + 3) & 3] = RD_KV(i + 3);
      acc = mfma32(xf[i], bb[i & 3], acc);
      SB();
    });
    STAMP(4);
    {
      v4u fr[2];
#pragma unroll
      for (int j = 0; j < 8; j++) fr[j >> 2][j & 3] = pk2(phi(acc[2 * j]), phi(acc[2 * j + 1]));
      lds_wr(sm, L_KVIN + w * 2048 + l16, fr[0]); lds_wr(sm, L_KVIN + w * 2048 + 1024 + l16, fr[1]);
    }
    SB(); asm volatile("s_waitcnt vmcnt(0) lgkmcnt(0)\n\ts_barrier" ::: "memory"); SB();
    STAMP(5);
    {
      v4u ka[2], vb[2][2];
      const int ko = L_KVIN + hd * 4096 + g4 * 512 + c16 * 16;
#pragma unroll
      for (int t_ = 0; t_ < 2; t_++) { ka[t_] = lds_rd(sm, ko + t_ * 2048 + (w & 1) * 256); vb[t_][0] = lds_rd(sm, ko + 8192 + t_ * 2048); vb[t_][1] = lds_rd(sm, ko + 8192 + t_ * 2048 + 256); }
      SB();
      f4 kv[2] = {};
#pragma unroll
      for (int t_ = 0; t_ < 2; t_++) { kv[0] = mfma16(ka[t_], vb[t_][0], kv[0]); kv[1] = mfma16(ka[t_], vb[t_][1], kv[1]); }
      stc(tag4(kv[0], tag), ws, part_off); stc(tag4(kv[1], tag), ws, part_off + 1024);
    }
    STAMP(6);
    if (w == 0) {
      v4u pv[8];
      const int so = OFF_PART + rh * 131072 + ((rj >> 3) * 2 + ((rj >> 1) & 1)) * 1024 + ((rj >> 2) & 1) * 256 + (rj & 1) * 128;
      __builtin_amdgcn_s_sleep(DLY_KV);
#pragma nounroll
      for (int spin = 0; spin < SPIN_CAP; spin++) {
        MEMBAR();
        const int vo = fresh(g4 * 4096 + (c16 >> 3) * 512 + (c16 & 7) * 16);
        unsigned mm = 0;
#pragma unroll
        for (int i = 0; i < 8; i++) pv[i] = ldf(ws, vo, so + i * 16384, 0);
#pragma unroll
        for (int i = 0; i < 8; i++) mm = tagx(mm, pv[i], tag);
        if (__builtin_amdgcn_ballot_w64((mm & 0xffu) != 0u) == 0) break;
        DBG(3);
      }
      f4 s = {};
#pragma unroll
      for (int i = 0; i < 8; i++) s += __builtin_bit_cast(f4, pv[i] & 0xffffff00u);
#pragma unroll
      for (int i = 0; i < 4; i++) s[i] = sum32(sum16(s[i]));
      const v2u o = {min(pk2(s[0], s[1]), SENT - 1u), min(pk2(s[2], s[3]), SENT - 1u)};
      __builtin_amdgcn_raw_buffer_store_b64(o, ws, l < 16 ? (c16 & 7) * 16 + (c16 >> 3) * 8 : OOB, OFF_KVF + rh * 2048 + rj * 128, SC1);
      STAMP(9);
    } else {
      __builtin_amdgcn_s_setprio(0);
#pragma nounroll
      for (int g = 15 + w; g < 56; g += 3) DMA_WO(g);
      if (w == 3) {
        wait_flags(ws, OFF_FS + xcd * 8192, ~0ull, l, tag, 0 DBGP);
        if (b == 0 && l == 0) __builtin_amdgcn_raw_buffer_store_b32(tag, ws, 0, OFF_EPOCH, SC1);
      } else asm volatile("s_waitcnt vmcnt(0)" ::: "memory");
      lds_arrive(sm, L_MISC + 8, l);
    }
    __builtin_amdgcn_s_setprio(0);
  } else {
    __builtin_amdgcn_s_setprio(0);
    sfor<13, 28>([&](auto II) {
      constexpr int i = decltype(II)::v;
      if constexpr (i == 13) set_flag(ws, OFF_FS, b, w == 7, l, tag);
      if constexpr (i == 21) { SB(); asm volatile("s_barrier" ::: "memory"); SB(); }
      if constexpr (i == 15 || i == 18 || i == 22 || i == 25) {
        constexpr int c = i == 15 ? 3 : (i == 18 ? 4 : (i == 22 ? 5 : 6));
        dma_q(wv, wsrd, (wrap(c + rot, 7) * 8 + w - 4) * 21504, lbase + L_X + (c * 8 + w - 4) * 896); DMA_Q(c);
      }
      if constexpr (i + 3 < 28) bb[(i + 3) & 3] = RD_KV(i + 3);
      acc = mfma32(xf[i], bb[i & 3], acc);
      SB();
    });
    STAMP(4);
    {
      const unsigned pad = r == 28 ? ONES : 0u;
      v4u fr[2];
#pragma unroll
      for (int j = 0; j < 8; j++) { const unsigned t_ = pk2(acc[2 * j], acc[2 * j + 1]); fr[j >> 2][j & 3] = r < 28 ? t_ : pad; }
      lds_wr(sm, L_KVIN + w * 2048 + l16, fr[0]); lds_wr(sm, L_KVIN + w * 2048 + 1024 + l16, fr[1]);
    }
    SB();
#pragma unroll
    for (int i = 0; i < 4; i++) DMA_WO(4 * i + w - 4);
    SB(); asm volatile("s_waitcnt vmcnt(4) lgkmcnt(0)\n\ts_barrier" ::: "memory"); SB();
    STAMP(5);
    const int qa = fresh(L_X + hh * 896 + trl);
    f16v aq = {};
#pragma unroll
    for (int i = 0; i < 3; i++) bb[i] = tr_frag(sm, qa + i * 1792);
    SB();
    sfor<0, 28>([&](auto II) {
      constexpr int i = decltype(II)::v;
      if constexpr (i + 3 < 28) bb[(i + 3) & 3] = tr_frag(sm, qa + (i + 3) * 1792);
      aq = mfma32(bb[i & 3], xf[i], aq);
      SB();
    });
    __builtin_amdgcn_s_setprio(3);
#pragma unroll
    for (int s = 0; s < 2; s++) {
      v4u pf = {pk2(phi(aq[8 * s]), phi(aq[8 * s + 1])), pk2(phi(aq[8 * s + 2]), phi(aq[8 * s + 3])), pk2(phi(aq[8 * s + 4]), phi(aq[8 * s + 5])), pk2(phi(aq[8 * s + 6]), phi(aq[8 * s + 7]))};
      if (s == 1) { pf[2] = hh ? 0u : pf[2]; pf[3] = hh ? 0u : pf[3]; }
      stc(canon(pf), ws, pq_off + s * 1024);
    }
    __builtin_amdgcn_s_setprio(0);
    STAMP(8);
    asm volatile("s_waitcnt vmcnt(2)" ::: "memory");
    lds_arrive(sm, L_MISC + 8, l);
  }
  lds_wait(lbase + L_MISC + 8, 7u);
  SB();
  STAMP(10);
  if (!ffn) {
    if (jcs < 28) {
      if (w < 7) {
        const rsrc_t ds = mk(p.wd, F * D * 2);
        unsigned v[4][8];
        const int dv = fresh(hh * 7168 + r * 4), so = 64 * jcs * 896 + 128 * w;
#pragma unroll
        for (int u = 0; u < 4; u++)
#pragma unroll
          for (int j = 0; j < 8; j++) v[u][j] = __builtin_amdgcn_raw_buffer_load_b32(ds, dv + (u & 1) * 896, so + (u >> 1) * 28672 + (j & 3) * 1792 + (j >> 2) * 14336, 0);
        SB();
#pragma unroll
        for (int u = 0; u < 4; u++) {
          const int d0 = OFF_WD + (224 * w + 4 * jcs + u) * 1024 + l16;
          stc(v4u{lo2(v[u][0], v[u][1]), lo2(v[u][2], v[u][3]), lo2(v[u][4], v[u][5]), lo2(v[u][6], v[u][7])}, ws, d0);
          stc(v4u{hi2(v[u][0], v[u][1]), hi2(v[u][2], v[u][3]), hi2(v[u][4], v[u][5]), hi2(v[u][6], v[u][7])}, ws, d0 + 114688);
        }
      }
      const int ft = 4 * jcs + (w >> 1), lo = fresh(l16);
      v4u v[14]; float cs = 0.f;
#pragma unroll
      for (int i = 0; i < 14; i++) v[i] = ldf(ws, lo, OFF_WC + (ft * 29 + 14 * (w & 1)) * 1024, i);
#pragma unroll
      for (int i = 0; i < 14; i++)
#pragma unroll
        for (int j = 0; j < 4; j++) cs = dot2(v[i][j], ONES, cs);
      cs = sum32(cs);
      *(float*)(sm + L_HST + w * 256 + l * 4) = cs;
      BAR();
      if (!(w & 1)) {
        cs += *(const float*)(sm + L_HST + (w + 1) * 256 + l * 4);
        const unsigned hi = pk2(cs, cs), lw = pk2(cs - bflo(hi), 0.f);
        const v4u fr = {l < 32 ? hi : 0u, l < 32 ? lw : 0u, 0u, 0u};
        stc(fr, ws, l16 + OFF_WC + (ft * 29 + 28) * 1024);
      }
      asm volatile("s_waitcnt vmcnt(0)" ::: "memory");
      BAR();
    }
    if (w == 0) set_flag(ws, OFF_FD, b, true, l, tag);
  } else {
    const int c4 = w & 3, th = w >> 2;
    v4u wo[16], pq[8], kvf[4], hv[8];
    int woa[2];
#pragma unroll
    for (int e = 0; e < 2; e++) woa[e] = L_WO + (16 * (g4 >> 1) + 4 * (g4 & 1) + (c16 >> 2)) * 128 + (((2 * c4 + ((c16 >> 1) & 1)) ^ (2 * ((((e + g4) & 1) << 1) + (c16 >> 3)))) * 16) + (c16 & 1) * 8;
    const int wo8 = g4 == 3 ? 0 : 1024;
    const int hoff = OFF_HF + (4 * tC * 28 + 4 * w) * 1024;
    v2u xr[2];
    {
      const int lo = fresh(l16);
#pragma unroll
      for (int i = 0; i < NPF0; i++) LD_WC(i);
      SB();
    }
    if (w != 0 && DLY_PQ) __builtin_amdgcn_s_sleep(DLY_PQ);
    __builtin_amdgcn_s_setprio(2);
    poll8(pq, ws, l16, OFF_PQ + ((4 * tB + a) * 32 + 4 * w) * 1024, OFF_PQ + ((4 * tB + a + 2) * 32 + 4 * w) * 1024, 4 DBGP, [&] {
      const int lo = fresh(l16);
      __builtin_amdgcn_s_setprio(0);
#pragma unroll
      for (int ps = 0; ps < 2; ps++) xr[ps] = *(const v2u*)((const char*)p.x + (size_t)((4 * tB + a + 2 * ps) * 32 + 16 * th + c16) * 896 + (64 * T + 16 * c4 + 4 * g4) * 2);
#pragma unroll
      for (int i = NPF0; i < NPF1; i++) LD_WC(i);
      SB();
#pragma unroll
      for (int i = 0; i < 16; i++) {
        const v2u x0 = tr_half(sm, woa[i & 1] + i * 3584), x1 = tr_half(sm, woa[i & 1] + wo8 + i * 3584);
        wo[i] = v4u{x0[0], x0[1], x1[0], x1[1]};
      }
    });
    STAMP(7);
    if (w != 0 && DLY_KF) __builtin_amdgcn_s_sleep(DLY_KF);
    __builtin_amdgcn_s_setprio(3);
    poll_frags<4>(kvf, ws, l16, OFF_KVF + 4 * w * 1024, 5 DBGP);
    __builtin_amdgcn_s_setprio(0);
    STAMP(11);
    sfor<0, 2>([&](auto PS) {
      constexpr int ps = decltype(PS)::v;
      const int lo = fresh(l16);
      f16v n[2] = {};
#pragma unroll
      for (int s = 0; s < 2; s++)
#pragma unroll
        for (int hd = 0; hd < 2; hd++) n[hd] = mfma32(kvf[2 * hd + s], pq[4 * ps + 2 * hd + s], n[hd]);
      constexpr int pa = ps ? NPF1 + 4 : NPF1;
      LD_WC(pa); LD_WC(pa + 1);
      SB();
#pragma unroll
      for (int hd = 0; hd < 2; hd++) {
        const auto sw = __builtin_amdgcn_permlane32_swap(__float_as_uint(n[hd][12]), __float_as_uint(n[hd][12]), false, false);
        const float inv = __builtin_amdgcn_rcpf(__uint_as_float(sw[1]) + 1e-6f);
        v4u o0, o1;
#pragma unroll
        for (int i = 0; i < 4; i++) { o0[i] = pk2(n[hd][2 * i] * inv, n[hd][2 * i + 1] * inv); o1[i] = pk2(n[hd][8 + 2 * i] * inv, n[hd][9 + 2 * i] * inv); }
        o1[2] = hh ? 0u : o1[2];
        lds_wr(sm, (ps ? L_OB1 : L_OB0) + (4 * w + 2 * hd) * 1024 + lo, o0);
        lds_wr(sm, (ps ? L_OB1 : L_OB0) + (4 * w + 2 * hd + 1) * 1024 + lo, o1);
      }
      BAR();
      if (ps == 0) STAMP(12);
      v4u ob[8];
      const int oo = fresh((ps ? L_OB1 : L_OB0) + g4 * 512 + th * 256 + c16 * 16), vh = fresh(w < 7 ? l16 : OOB);
#pragma unroll
      for (int i = 0; i < 8; i++) ob[i] = lds_rd(sm, oo + i * 2048);
      f4 hacc = {bflo(xr[ps][0]), bfhi(xr[ps][0]), bflo(xr[ps][1]), bfhi(xr[ps][1])};
      SB();
#pragma unroll
      for (int i = 0; i < 16; i++) {
        hacc = mfma16(wo[i], ob[i & 7], hacc);
        if (i < 8) ob[i] = lds_rd(sm, oo + (8 + i) * 2048);
        if (ps == 1 && (i & 1)) hv[i >> 1] = ldf(ws, vh, hoff + (i >> 3) * 28 * 1024, (i >> 1) & 3);
        SB();
      }
      const unsigned p0 = min(pk2(hacc[0], hacc[1]), SENT - 1u), p1 = min(pk2(hacc[2], hacc[3]), SENT - 1u);
      const auto s0 = __builtin_amdgcn_permlane16_swap(p0, p0, false, false), s1 = __builtin_amdgcn_permlane16_swap(p1, p1, false, false);
      const v4u hf = {s0[0], s1[0], s0[1], s1[1]};
      stc(hf, ws, (g4 & 1) ? OOB : OFF_HF + ((4 * tB + a + 2 * ps) * 28 + 4 * T + c4) * 1024 + (g4 >> 1) * 512 + th * 256 + c16 * 16);
      STAMP(13 + ps);
      LD_WC(pa + 2); LD_WC(pa + 3);
      {
        const float s_ = dot2(p0, ONES, dot2(p1, ONES, 0.f)), ss_ = dot2(p0, p0, dot2(p1, p1, 0.f));
        *(f2*)(sm + L_XN + ((c4 * 4 + g4) * 32 + 16 * th + c16) * 16 + ps * 8) = f2{s_, ss_};
      }
      SB();
    });
    v4u wd[28], av[14];
    const int rd = 2 * wrap(wrap(T + 2 * (2 * (idx >> 4) + a), 7), 7);
    const int wd_off = OFF_WD + ((2 * T) * 112 + 14 * w) * 1024;
#define LD_WD(i) wd[i] = ldf(ws, (ABL & 1) ? OOB : lo, wd_off + (((i) & 1) * 112 + wrap((((i) >> 2) << 1) + rd, 14)) * 1024, ((i) >> 1) & 1)
#define LD_AV(i, off) av[i] = ldf(ws, (ABL & 4) ? OOB : lo, (off) + wrap(((i) & ~1) + rd, 14) * 1024, (i) & 1)
    const int g2a = w < 7 ? hoff + 56 * 1024 : OFF_HS + tC * 8192, g2b = w < 7 ? hoff + 84 * 1024 : OFF_HS + tC * 8192 + 3 * 1024;
    int hw[4];
#pragma unroll
    for (int i = 0; i < 4; i++) hw[i] = w < 7 ? wrap(4 * w + i + 28 - 7 * rq, 28) * 1024 : L_HST - L_H1 + i * 1024;
    const int hk = w < 7 ? 28 * 1024 : 3 * 1024;
    float c1[2], vv[2]; unsigned mneg[2];
    auto get_stats = [&](int ps) -> float {
      float s_ = 0.f, ss_ = 0.f;
#pragma unroll
      for (int i = 0; i < 7; i++) { const f2 q = *(const f2*)(sm + L_HST + i * 1024 + l16 + ps * 8); s_ += q[0]; ss_ += q[1]; }
      const float nm = s_ * (-1.f / D), ve = fmaxf(ss_ * (1.f / D) - nm * nm, 0.f) + 1e-5f;
      const float cc = __builtin_amdgcn_rsqf(ve) * -1.44269504089f;
      const unsigned mh = pk2(nm, 0.f), mm = pk2(nm, nm - bflo(mh));
      const auto s1 = __builtin_amdgcn_permlane32_swap(__float_as_uint(cc), __float_as_uint(cc), false, false);
      const auto s2 = __builtin_amdgcn_permlane32_swap(__float_as_uint(ve), __float_as_uint(ve), false, false);
      const auto s3 = __builtin_amdgcn_permlane32_swap(mm, mm, false, false);
#pragma unroll
      for (int k = 0; k < 2; k++) { c1[k] = __uint_as_float(s1[k]); vv[k] = __uint_as_float(s2[k]); mneg[k] = s3[k]; }
      return s_;
    };
    BAR();
    asm volatile("" : "+v"(hv[0]), "+v"(hv[1]), "+v"(hv[2]), "+v"(hv[3]), "+v"(hv[4]), "+v"(hv[5]), "+v"(hv[6]), "+v"(hv[7]));
    if (w < 7) {
      unsigned mm = 0;
#pragma unroll
      for (int i = 0; i < 8; i++) mm = mx4(mm, hv[i]);
      if (bad(mm)) { DBG(6); poll8(hv, ws, l16, hoff, hoff + 28 * 1024, 6 DBGP, [] {}); }
      STAMP(15);
#pragma unroll
      for (int i = 0; i < 8; i++) lds_wr(sm, L_H0 + l16 + hw[i & 3] + (i >> 2) * 28 * 1024, hv[i]);
    } else {
      f4 t = {};
#pragma unroll
      for (int i = 0; i < 8; i++) t += *(const f4*)(sm + L_XN + i * 1024 + l16);
#pragma unroll
      for (int i = 0; i < 4; i++) t[i] = sum32(t[i]);
      stc(canon(__builtin_bit_cast(v4u, t)), ws, l < 32 ? l16 + OFF_HS + ((tB * 8 + T) * 2 + a) * 512 : OOB);
    }
    BAR();
    STAMP(16);
    const int ftt = 4 * tB + a + 2 * (w >> 2);
    v4u res;
    const int a0off = OFF_ACT + ((4 * tB + a) * 112 + 14 * w) * 1024;
    const srd_t srd = mks(p.ws, WS_BYTES);
    const int fdo = (28 + (g4 & 1) * 32 + (c16 >> 2)) * 128 + (l & 3) * 4;
    sfor<0, 2>([&](auto PS) {
      constexpr int ps = decltype(PS)::v;
      f16v ac[2] = {};
      if constexpr (ps == 1) get_stats(1);
      const int lo = fresh(l16);
      const int hb = fresh((ps ? L_H1 : L_H0) + l16);
      v4u bb[3][2];
      unsigned fa = 0, mm = 0;
#pragma unroll
      for (int i = 0; i < 2; i++) { bb[i][0] = lds_rd(sm, hb + i * 1024); bb[i][1] = lds_rd(sm, hb + (28 + i) * 1024); }
      if constexpr (ps == 1) {
#pragma unroll
        for (int k = 0; k < 2; k++) { const v4u bx = {mneg[k], mneg[k] & 0xffffu, 0u, 0u}; ac[k] = mfma32(wc[28], bx, ac[k]); }
      }
      SB();
      sfor<0, 28>([&](auto II) {
        constexpr int i = decltype(II)::v;
        if constexpr (i % 7 == 0) __builtin_amdgcn_s_setprio(3 - i / 7);
        if constexpr (i % 4 == 0) STAMPX(1 + ps, 1 + i / 4);
        if constexpr (i == 23) STAMPX(1 + ps, 8);
#pragma unroll
        for (int k = 0; k < 2; k++) {
          if constexpr (i + 2 < 28) bb[(i + 2) % 3][k] = lds_rd(sm, hb + (k * 28 + i + 2) * 1024);
          ac[k] = mfma32(wc[i], bb[i % 3][k], ac[k]);
          if constexpr (ps == 0 && i >= 20 && i < 24) {
            constexpr int f0 = 2 * (i - 20);
            mm = mx4(mm, hv[f0 + k]);
            lds_wr(sm, L_H1 + lo + hw[(f0 + k) & 3] + ((f0 + k) >> 2) * hk, hv[f0 + k]);
          }
          SB();
        }
        if constexpr (ps == 0) {
          if constexpr (NPF1 + 8 + i < 28) LD_WC(NPF1 + 8 + i);
          if constexpr (i >= 10 && i < 14) { hv[2 * (i - 10)] = ldf(ws, lo, i < 12 ? g2a : g2b, (2 * (i - 10)) & 3); hv[2 * (i - 10) + 1] = ldf(ws, lo, i < 12 ? g2a : g2b, (2 * (i - 10) + 1) & 3); }
          if constexpr (i == 8) fa = __builtin_amdgcn_raw_buffer_load_b32(ws, fdo, OFF_FD + xcd * 8192, SC1);
          if constexpr (i == 15) { if (__builtin_amdgcn_ballot_w64(fa != tag) != 0) DBG(2); wait_words(fa, tag, srd, fdo, OFF_FD + xcd * 8192); wc[28] = ldf(ws, lo, wc_off + 28 * 1024, 0); }
          if constexpr (i == 16 || i == 18) stc(sent, ws, lo + OFF_ACT + ((4 * tC + i / 2 - 6) * 112 + 8 * cC + w) * 1024);
          if constexpr (i >= 28 - NWD0) LD_WD(i - (28 - NWD0));
          if constexpr (i == 23) { *(unsigned*)(sm + L_MISC + (mm == SENT ? 12 : 16)) = 1u; SB(); asm volatile("s_waitcnt lgkmcnt(0)\n\ts_barrier" ::: "memory"); }
          if constexpr (i == 25) { get_stats(0); fa = *(const unsigned*)(sm + L_MISC + 12); }
          SB();
        } else {
          if constexpr (NWD0 + i < 28) LD_WD(NWD0 + i);
          if constexpr (i == 7) {
            SB(); asm volatile("s_waitcnt vmcnt(8)\n\ts_barrier" ::: "memory"); SB();
            __builtin_amdgcn_raw_buffer_store_b32(tag, ws, threadIdx.x == 0 ? 0 : OOB, OFF_FA + (tC * 16 + cC) * 128, SC1);
          }
          if constexpr (i == 22) res = ldf(ws, lo, OFF_HF + (ftt * 28 + 4 * T + (w & 3)) * 1024, 0);
          if constexpr (i == 17) fa = __builtin_amdgcn_raw_buffer_load_b32(ws, l < 14 ? l * 128 : 0, OFF_FA + tB * 2048, SC1);
          if constexpr (i == 23) { if (__builtin_amdgcn_ballot_w64(fa != tag) != 0) DBG(10); }
          if constexpr (i == 23) wait_words(fa, tag, srd, l < 14 ? l * 128 : 0, OFF_FA + tB * 2048);
          if constexpr (i >= 23) { LD_AV(2 * (i - 23), a0off); LD_AV(2 * (i - 23) + 1, a0off); }
          SB();
        }
      });
      if constexpr (ps == 0) {
        if (__builtin_amdgcn_readfirstlane(fa) != 0u) {
          DBG(7);
          BAR();
          poll8(hv, ws, l16, g2a, g2b, 8 DBGP, [] {});
#pragma unroll
          for (int f = 0; f < 8; f++) lds_wr(sm, L_H1 + l16 + hw[f & 3] + (f >> 2) * hk, hv[f]);
          BAR();
          get_stats(0);
        }
#pragma unroll
        for (int k = 0; k < 2; k++) { const v4u bx = {mneg[k], mneg[k] & 0xffffu, 0u, 0u}; ac[k] = mfma32(wc[28], bx, ac[k]); }
      }
      STAMP(17 + 2 * ps);
#pragma unroll
      for (int k = 0; k < 2; k++) {
        float o[8];
#pragma unroll
        for (int j = 0; j < 8; j++) {
          const float e = __builtin_amdgcn_exp2f(ac[k][j] * c1[k]);
          o[j] = ac[k][j] * ac[k][8 + j] * __builtin_amdgcn_rcpf(__builtin_fmaf(e, vv[k], vv[k]));
        }
        const v4u af = {pk2(o[0], o[1]), pk2(o[2], o[3]), pk2(o[4], o[5]), pk2(o[6], o[7])};
        stc(canon(af), ws, l16 + OFF_ACT + ((4 * tC + 2 * ps + k) * 112 + 8 * cC + w) * 1024);
      }
      STAMP(18 + 2 * ps);
    });
    {
      int lo = fresh(l16);
      LD_AV(10, a0off); LD_AV(11, a0off); LD_AV(12, a0off); LD_AV(13, a0off);
      SB();
      const rsrc_t os = mk(p.out, S * D * 2);
      const int oo = (ftt * 32 + r) * 896 + (64 * T + 16 * (w & 3) + 8 * (l >> 5)) * 2;
      {
        f16v acc0 = {}, acc1 = {};
        if (w >= 4) asm volatile("s_barrier" ::: "memory");
        SB();
#pragma unroll
        for (int i = 0; i < 14; i++) { acc0 = mfma32(wd[2 * i], av[i], acc0); acc1 = mfma32(wd[2 * i + 1], av[i], acc1); SB(); }
        STAMP(21);
        if (w < 4) { red_write(sm, L_H0 + w * 8192 + l16, acc0, acc1); asm volatile("s_barrier" ::: "memory"); __builtin_amdgcn_s_sleep(DLY_AV); }
        SB();
        lo = fresh(l16);
#pragma unroll
        for (int i = 0; i < 14; i++) LD_AV(i, a0off + 2 * 112 * 1024);
        SB();
        if (w >= 4) red_write(sm, L_H0 + w * 8192 + l16, acc0, acc1);
        BAR();
        v4u of = res;
        if (w < 4) { asm volatile("" : "+v"(of)); of = red_final(sm, L_H0 + (w & 3) * 1024 + l16, of); }
        __builtin_amdgcn_raw_buffer_store_b128(of, os, w < 4 ? oo : OOB, 0, 0);
        SB();
      }
      {
        f16v acc0 = {}, acc1 = {};
#pragma unroll
        for (int i = 0; i < 14; i++) { if (i == 0) __builtin_amdgcn_s_setprio(3); if (i == 4) __builtin_amdgcn_s_setprio(2); if (i == 8) __builtin_amdgcn_s_setprio(1); if (i == 12) __builtin_amdgcn_s_setprio(0); acc0 = mfma32(wd[2 * i], av[i], acc0); acc1 = mfma32(wd[2 * i + 1], av[i], acc1); SB(); }
        if (__builtin_amdgcn_ballot_w64(acc0[0] != acc0[0]) != 0) {
          DBG(11);
#pragma nounroll
          for (int spin = 0; spin < SPIN_CAP; spin++) {
            MEMBAR();
            unsigned mm = 0;
            lo = fresh(l16);
#pragma unroll
            for (int i = 0; i < 14; i++) LD_AV(i, a0off + 2 * 112 * 1024);
#pragma unroll
            for (int i = 0; i < 14; i++) mm = mx4(mm, av[i]);
            if (!bad(mm)) break;
          }
          acc0 = f16v{}; acc1 = f16v{};
#pragma unroll
          for (int i = 0; i < 14; i++) { acc0 = mfma32(wd[2 * i], av[i], acc0); acc1 = mfma32(wd[2 * i + 1], av[i], acc1); }
        }
        STAMP(22);
        red_write(sm, L_H1 + w * 8192 + l16, acc0, acc1);
        BAR();
        if (w >= 4) {
          v4u rr = res; asm volatile("" : "+v"(rr));
          __builtin_amdgcn_raw_buffer_store_b128(red_final(sm, L_H1 + (w & 3) * 1024 + l16, rr), os, oo, 0, 0);
        }
      }
    }
  }
  asm volatile(".Lfb_end:");
  STAMP(23);
}

extern "C" void kernel_launch(void* const* d_in, const int* in_sizes, int n_in, void* d_out, int out_size, void* d_ws, size_t ws_size, hipStream_t stream) {
  Ptrs p;
  p.x = d_in[0]; p.wqkv = d_in[1]; p.wo = d_in[2]; p.wg = d_in[4]; p.wu = d_in[5]; p.wd = d_in[6]; p.out = (char*)d_out; p.ws = (char*)d_ws;
  fused_block<<<256, 512, 0, stream>>>(p);
}
```

```cpp
#include <hip/hip_runtime.h>
#include <stdint.h>

constexpr int S = 2048, D = 448, F = 1792;
typedef unsigned v4u __attribute__((ext_vector_type(4)));
typedef unsigned v2u __attribute__((ext_vector_type(2)));
typedef float f4 __attribute__((ext_vector_type(4)));
typedef float f2 __attribute__((ext_vector_type(2)));
typedef float f16v __attribute__((ext_vector_type(16)));
typedef __bf16 bf2 __attribute__((ext_vector_type(2)));
typedef __bf16 bf8 __attribute__((ext_vector_type(8)));
typedef int srd_t __attribute__((ext_vector_type(4)));
typedef __amdgpu_buffer_rsrc_t rsrc_t;
#define DI __device__ __forceinline__
#define SB() __builtin_amdgcn_sched_barrier(0)
#define MEMBAR() asm volatile("" ::: "memory")
#define BAR() do { SB(); asm volatile("s_waitcnt lgkmcnt(0)\n\ts_barrier" ::: "memory"); SB(); } while (0)

constexpr int OFF_EPOCH = 0, OFF_FA = 32768;
constexpr int OFF_FS = 65536, OFF_FC = OFF_FS + 65536, OFF_FD = OFF_FC + 65536;
constexpr int OFF_WC = 262144;
constexpr int OFF_WD = OFF_WC + 112 * 29 * 1024;
constexpr int OFF_PART = OFF_WD + 1568 * 1024;
constexpr int OFF_KVF = OFF_PART + 2048 * 1024;
constexpr int OFF_PQ = OFF_KVF + 32 * 1024;
constexpr int OFF_HF = OFF_PQ + 2048 * 1024;
constexpr int OFF_HS = OFF_HF + 1792 * 1024;
constexpr int OFF_ACT = OFF_HS + 128 * 1024;
constexpr int WS_BYTES = OFF_ACT + 7168 * 1024;
constexpr int OOB = 0x40000000;
constexpr int SC1 = 16;
constexpr unsigned SENT = 0xFFFFFFFFu;
constexpr int SPIN_CAP = 1 << 16;

#define STAMP(i)
#define STAMPX(set, i)
#define DBG(i)
#define DBGA
#define DBGP

DI rsrc_t mk(const void* p, int bytes) { return __builtin_amdgcn_make_buffer_rsrc((void*)p, 0, bytes, 0x00020000); }
DI v4u ldf(rsrc_t r, int voff, int base, int i) { return __builtin_amdgcn_raw_buffer_load_b128(r, voff + (i & 3) * 1024, base + (i >> 2) * 4096, SC1); }
DI void stc(v4u v, rsrc_t r, int off) { __builtin_amdgcn_raw_buffer_store_b128(v, r, off, 0, SC1); }
DI v4u canon(v4u v) { v4u o; for (int i = 0; i < 4; i++) o[i] = min(v[i], SENT - 1u); return o; }
DI f16v mfma32(v4u a, v4u b, f16v c) { return __builtin_amdgcn_mfma_f32_32x32x16_bf16(__builtin_bit_cast(bf8, a), __builtin_bit_cast(bf8, b), c, 0, 0, 0); }
DI f4 mfma16(v4u a, v4u b, f4 c) { return __builtin_amdgcn_mfma_f32_16x16x32_bf16(__builtin_bit_cast(bf8, a), __builtin_bit_cast(bf8, b), c, 0, 0, 0); }
DI unsigned pk2(float lo, float hi) { f2 v = {lo, hi}; return __builtin_bit_cast(unsigned, __builtin_convertvector(v, bf2)); }
DI float dot2(unsigned a, unsigned b, float c) { return __builtin_amdgcn_fdot2_f32_bf16(__builtin_bit_cast(bf2, a), __builtin_bit_cast(bf2, b), c, false); }
DI float bflo(unsigned u) { return __uint_as_float(u << 16); }
DI float bfhi(unsigned u) { return __uint_as_float(u & 0xffff0000u); }
DI float phi(float t) { return (fmaxf(t, 0.f) + 1e-6f) + __builtin_amdgcn_fmed3f(__builtin_amdgcn_exp2f(t * 1.44269504089f), 0.f, 1.f); }
constexpr unsigned ONES = 0x3f803f80u;
DI v4u lds_rd(const char* sm, int off) { return *(const v4u*)(sm + off); }
DI void lds_wr(char* sm, int off, v4u v) { *(v4u*)(sm + off) = v; }
DI v4u f4u(float a, float b, float c, float d) { v4u r = {__float_as_uint(a), __float_as_uint(b), __float_as_uint(c), __float_as_uint(d)}; return r; }
DI unsigned mx4(unsigned m, v4u v) { return max(max(max(m, v[0]), v[1]), max(v[2], v[3])); }
DI bool bad(unsigned m) { return __builtin_amdgcn_ballot_w64(m == SENT) != 0; }
DI float sum16(float v) { auto s = __builtin_amdgcn_permlane16_swap(__float_as_uint(v), __float_as_uint(v), false, false); return __uint_as_float(s[0]) + __uint_as_float(s[1]); }
DI float sum32(float v) { auto s = __builtin_amdgcn_permlane32_swap(__float_as_uint(v), __float_as_uint(v), false, false); return __uint_as_float(s[0]) + __uint_as_float(s[1]); }
template <int CTRL> DI float dpp_add(float v) { return v + __builtin_bit_cast(float, __builtin_amdgcn_update_dpp(0, __builtin_bit_cast(int, v), CTRL, 0xf, 0xf, true)); }
DI float wave_sum(float v) { v = dpp_add<0xB1>(v); v = dpp_add<0x4E>(v); v = dpp_add<0x141>(v); v = dpp_add<0x140>(v); return sum32(sum16(v)); }
DI int fresh(int v) { asm volatile("" : "+v"(v)); return v; }
#define PIN(x) asm volatile("" : "+v"(x))
DI int sfresh(int v) { asm volatile("" : "+s"(v)); return v; }
DI int wrap(int v, int n) { return v >= n ? v - n : v; }
template <int I> struct IC { static constexpr int v = I; };
template <int I, int N, class Fn> DI void sfor(Fn&& f) { if constexpr (I < N) { f(IC<I>{}); sfor<I + 1, N>(f); } }

struct Ptrs { const void *x, *wqkv, *wo, *wg, *wu, *wd; char* out; char* ws; };

DI srd_t mks(const void* p, int bytes) { return srd_t{(int)(size_t)p, (int)((size_t)p >> 32) & 0xffff, bytes, 0x00020000}; }
DI v4u tag4(f4 v, unsigned tag) { v4u o; for (int i = 0; i < 4; i++) o[i] = __builtin_amdgcn_perm(__float_as_uint(v[i]), tag, 0x07060500u); return o; }
DI unsigned tagx(unsigned m, v4u v, unsigned tag) { return m | ((v[0] ^ tag) | (v[1] ^ tag)) | ((v[2] ^ tag) | (v[3] ^ tag)); }
DI unsigned lo2(unsigned a, unsigned b) { return __builtin_amdgcn_perm(b, a, 0x05040100u); }
DI unsigned hi2(unsigned a, unsigned b) { return __builtin_amdgcn_perm(b, a, 0x07060302u); }
DI void wait_flags(rsrc_t ws, int off, unsigned long long lines, int l, unsigned tag, int id DBGA) {
  const int vo = ((lines >> l) & 1) ? l * 128 : OOB;
#pragma nounroll
  for (int spin = 0; spin < SPIN_CAP; spin++) {
    MEMBAR();
    const v4u f = ldf(ws, vo, off, 0);
    if ((__builtin_amdgcn_ballot_w64(((f[0] ^ tag) | (f[1] ^ tag) | (f[2] ^ tag) | (f[3] ^ tag)) != 0u) & lines) == 0) break;
    DBG(id);
    __builtin_amdgcn_s_sleep(2);
  }
}
DI void set_flag(rsrc_t ws, int off, int b, bool on, int l, unsigned tag) {
  __builtin_amdgcn_raw_buffer_store_b32(tag, ws, (on && l < 8) ? l * 8192 : OOB, off + (b >> 2) * 128 + (b & 3) * 4, SC1);
}
DI void wait_words(unsigned v, unsigned tag, srd_t srd, int voff, int soff) {
  int cnt;
  asm volatile("s_mov_b32 %1, 0x10000\n"
    "1:\n\tv_cmp_ne_u32 vcc, %0, %2\n\ts_cbranch_vccz 2f\n\t"
    "buffer_load_dword %0, %3, %4, %5 offen sc1\n\ts_waitcnt vmcnt(0)\n\t"
    "s_sub_u32 %1, %1, 1\n\ts_cbranch_scc0 1b\n"
    "2:" : "+v"(v), "=&s"(cnt) : "v"(tag), "v"(voff), "s"(srd), "s"(soff) : "vcc", "scc", "memory");
}
template <int N> DI void poll_frags(v4u (&v)[N], rsrc_t ws, int voff, int base, int id DBGA) {
#pragma nounroll
  for (int spin = 0; spin < SPIN_CAP; spin++) {
    MEMBAR();
    const int lo = fresh(voff);
    unsigned m = 0;
#pragma unroll
    for (int i = 0; i < N; i++) v[i] = ldf(ws, lo, base, i);
#pragma unroll
    for (int i = 0; i < N; i++) m = mx4(m, v[i]);
    if (!bad(m)) break;
    DBG(id);
  }
}
template <class Fn> DI void poll8(v4u (&v)[8], rsrc_t ws, int voff, int s0, int s1, int id DBGA, Fn&& hook) {
  {
    const int lo = fresh(voff);
    unsigned m = 0;
#pragma unroll
    for (int i = 0; i < 8; i++) v[i] = ldf(ws, lo, i < 4 ? s0 : s1, i & 3);
    SB(); hook(); SB();
#pragma unroll
    for (int i = 0; i < 8; i++) m = mx4(m, v[i]);
    if (!bad(m)) return;
    DBG(id);
  }
#pragma nounroll
  for (int spin = 0; spin < SPIN_CAP; spin++) {
    MEMBAR();
    const int lo = fresh(voff);
    unsigned m = 0;
#pragma unroll
    for (int i = 0; i < 8; i++) v[i] = ldf(ws, lo, i < 4 ? s0 : s1, i & 3);
#pragma unroll
    for (int i = 0; i < 8; i++) m = mx4(m, v[i]);
    if (!bad(m)) break;
    DBG(id);
  }
}
typedef short s4v __attribute__((ext_vector_type(4)));
#define LAS __attribute__((address_space(3)))
DI v4u tr_frag(const char* sm, int off) {
  const v2u a = __builtin_bit_cast(v2u, __builtin_amdgcn_ds_read_tr16_b64_v4i16((LAS s4v*)((LAS char*)sm + off)));
  const v2u b = __builtin_bit_cast(v2u, __builtin_amdgcn_ds_read_tr16_b64_v4i16((LAS s4v*)((LAS char*)sm + off + 448)));
  return v4u{a[0], a[1], b[0], b[1]};
}
DI void dma_kv(int voff, srd_t srd, int soff, int lds) {
  unsigned keep;
  asm volatile("s_mov_b32 %0, m0\n\ts_mov_b32 exec_hi, 0x00ffffff\n\ts_mov_b32 m0, %4\n\ts_nop 0\n\t"
               "buffer_load_dwordx4 %1, %2, %3 offen offset:896 lds\n\tbuffer_load_dwordx4 %1, %2, %3 offen offset:1792 lds\n\t"
               "s_mov_b32 m0, %0\n\ts_mov_b32 exec_hi, -1" : "=&s"(keep) : "v"(voff), "s"(srd), "s"(soff), "s"(lds - 896) : "memory");
}
DI void dma_q(int voff, srd_t srd, int soff, int lds) {
  unsigned keep;
  asm volatile("s_mov_b32 %0, m0\n\ts_mov_b32 exec_hi, 0x00ffffff\n\ts_mov_b32 m0, %4\n\ts_nop 0\n\t"
               "buffer_load_dwordx4 %1, %2, %3 offen lds\n\t"
               "s_mov_b32 m0, %0\n\ts_mov_b32 exec_hi, -1" : "=&s"(keep) : "v"(voff), "s"(srd), "s"(soff), "s"(lds) : "memory");
}
DI void lds_arrive(char* sm, int off, int l) { if (l == 0) __hip_atomic_fetch_add((unsigned*)(sm + off), 1u, __ATOMIC_RELAXED, __HIP_MEMORY_SCOPE_WORKGROUP); }
DI void lds_wait(int addr, unsigned n) {
  unsigned t;
  asm volatile("1:\n\tds_read_b32 %0, %1\n\ts_waitcnt lgkmcnt(0)\n\tv_cmp_gt_u32 vcc, %2, %0\n\ts_cbranch_vccnz 1b" : "=&v"(t) : "v"(addr), "s"(n) : "vcc", "memory");
}
DI void dma_full(int voff, srd_t srd, int soff, int lds) {
  unsigned keep;
  asm volatile("s_mov_b32 %0, m0\n\ts_mov_b32 m0, %4\n\ts_nop 0\n\tbuffer_load_dwordx4 %1, %2, %3 offen lds\n\ts_mov_b32 m0, %0" : "=&s"(keep) : "v"(voff), "s"(srd), "s"(soff), "s"(lds) : "memory");
}
DI v2u tr_half(const char* sm, int off) { return __builtin_bit_cast(v2u, __builtin_amdgcn_ds_read_tr16_b64_v4i16((LAS s4v*)((LAS char*)sm + off))); }
DI void red_write(char* sm, int off, const f16v& a0, const f16v& a1) {
#pragma unroll
  for (int q = 0; q < 4; q++) {
    lds_wr(sm, off + q * 1024, f4u(a0[4 * q], a0[4 * q + 1], a0[4 * q + 2], a0[4 * q + 3]));
    lds_wr(sm, off + (4 + q) * 1024, f4u(a1[4 * q], a1[4 * q + 1], a1[4 * q + 2], a1[4 * q + 3]));
  }
}
DI v4u red_final(const char* sm, int off, v4u res) {
  v4u t[16];
#pragma unroll
  for (int i = 0; i < 16; i++) t[i] = lds_rd(sm, off + i * 4096);
  SB();
  f4 e = {bflo(res[0]), bflo(res[1]), bflo(res[2]), bflo(res[3])}, o = {bfhi(res[0]), bfhi(res[1]), bfhi(res[2]), bfhi(res[3])};
#pragma unroll
  for (int w = 0; w < 8; w++) { e += __builtin_bit_cast(f4, t[2 * w]); o += __builtin_bit_cast(f4, t[2 * w + 1]); }
  v4u out;
#pragma unroll
  for (int i = 0; i < 4; i++) out[i] = pk2(e[i], o[i]);
  return out;
}

constexpr int L_X = 0;
constexpr int L_KV = 57344;
constexpr int L_KVIN = 114688;
constexpr int L_WO = 57344;
constexpr int L_OB0 = 0, L_OB1 = 114688;
constexpr int L_H0 = 0;
constexpr int L_H1 = 65536;
constexpr int L_HST = 131328;
constexpr int L_XN = 149504;
constexpr int L_MISC = 157696;
constexpr int L_BYTES = 157824;

#define ABL 0
#define DLY_X 20
#define DLY_AV 10
#define AV2AT 11
#define WARM 0
#define ROTD 0
#define ROTC 0
#define DLY_PQ 0
#define DLY_KF 0
#define DLY_F1 8
#define DLY_KV 16
#define NPF0 4
#define NPF1 10
#define NPF2 9
#define NPF3 15
#define NWD0 4

__global__ __launch_bounds__(512, 2) void fused_block(Ptrs p) {
  __shared__ __attribute__((aligned(16))) char sm[L_BYTES];
  STAMP(0);
  const rsrc_t ws = mk(p.ws, WS_BYTES);
  const int b = blockIdx.x, xcd = b & 7, idx = b >> 3;
  const int w = __builtin_amdgcn_readfirstlane(threadIdx.x >> 6), l = threadIdx.x & 63, l16 = l * 16;
  const int t64 = 8 * (xcd & 3) + (idx & 7), hp = 4 * (xcd >> 2) + (idx >> 3);
  const int rot = wrap((idx & 7) + 2 * (idx >> 3), 7) % 7;
  v4u xg[7];
  {
    const srd_t xs = mks(p.x, S * D * 2);
    const int xv = (l >> 3) * 896 + (l & 7) * 16, xso = (t64 * 64 + w * 8) * 896;
#pragma unroll
    for (int i = 0; i < 7; i++) asm volatile("buffer_load_dwordx4 %0, %1, %2, %3 offen" : "=v"(xg[i]) : "v"(xv), "s"(xs), "s"(xso + wrap(i + rot, 7) * 128) : "memory");
  }
  unsigned ep;
  {
    unsigned long long pc; int len;
    asm volatile("s_getpc_b64 %0\n.Lfb_pc:\n\ts_mov_b32 %1, .Lfb_end-.Lfb_pc" : "=s"(pc), "=s"(len));
    const srd_t es = mks(w < 4 ? (const void*)p.ws : (const void*)pc, w == 0 ? 64 : ((idx & 3) == w - 4 ? len : 0));
    asm volatile("buffer_load_dword %0, %1, %2, 0 offen sc1\n\ts_barrier" : "=v"(ep) : "v"(w < 4 ? 0 : l * 128 + (w - 4) * 8192), "s"(es) : "memory");
  }
  const srd_t wsrd = mks(p.wqkv, D * 3 * D * 2);
  const int l7 = (l * 37) >> 8, wv = l7 * 2688 + (l - 7 * l7) * 16 + hp * 112;
  const int lbase = (int)(size_t)(LAS char*)sm;
#define DMA_KV(c) dma_kv(wv, wsrd, (wrap((c) + rot, 7) * 8 + w) * 21504, lbase + L_KV + ((c) * 8 + w) * 1792)
#define DMA_Q(c) dma_q(wv, wsrd, (wrap((c) + rot, 7) * 8 + w) * 21504, lbase + L_X + ((c) * 8 + w) * 896)
  const int tB = 2 * xcd + (idx >> 4), m = idx & 15, T = m >> 1, a = m & 1;
  const bool ffn = m < 14;
  const int r = l & 31, c16 = l & 15, g4 = l >> 4, hh = l >> 5;
  const v4u sent = {SENT, SENT, SENT, SENT};
  const int tt = w & 1, hd = (w >> 1) & 1;
  const int rh = b >> 4, rj = b & 15;
  const int pq_off = OFF_PQ + ((2 * t64 + tt) * 16 + 2 * hp + hd) * 2048 + l16;
  const int part_off = OFF_PART + ((2 * hp + hd) * 32 + t64) * 4096 + (w & 1) * 2048 + l16;
  DMA_KV(0);
  unsigned ut[8];
  const int uks = 7 * ((b >> 1) & 3) + (w ? w - 1 : 0);
    {
      const srd_t us = mks((b & 1) ? p.wu : p.wg, D * F * 2);
      const int uv = (b < 224 && w) ? hh * 28672 + r * 4 : OOB, uo = uks * 57344 + idx * 128;
#pragma unroll
      for (int j = 0; j < 8; j++) asm volatile("buffer_load_dword %0, %1, %2, %3 offen" : "=v"(ut[j]) : "v"(uv), "s"(us), "s"(uo + j * 3584) : "memory");
    }
  SB();
  const int ai = (idx >> 4) * 14 + m, tC = 4 * (xcd & 3) + (ai & 3), cC = 7 * (xcd >> 2) + (ai >> 2);
  const int jcs = xcd * 4 + (idx >> 4) * 2 + (m - 14);
  *(v2u*)(sm + L_MISC + (threadIdx.x == 0 ? 8 : 16)) = v2u{0u, 0u};
  SB();
  asm volatile("s_waitcnt vmcnt(10)" : "+v"(ep), "+v"(xg[0]), "+v"(xg[1]), "+v"(xg[2]), "+v"(xg[3]), "+v"(xg[4]), "+v"(xg[5]), "+v"(xg[6]));
  STAMP(1);
  DMA_KV(1);
  {
    float s = 0.f, ss = 0.f;
#pragma unroll
    for (int i = 0; i < 7; i++)
#pragma unroll
      for (int j = 0; j < 4; j++) { s = dot2(xg[i][j], ONES, s); ss = dot2(xg[i][j], xg[i][j], ss); }
    SB(); DMA_KV(2); SB();
    s = dpp_add<0x141>(dpp_add<0x4E>(dpp_add<0xB1>(s))); ss = dpp_add<0x141>(dpp_add<0x4E>(dpp_add<0xB1>(ss)));
    const float mean = s * (1.f / D), var = fmaxf(ss * (1.f / D) - mean * mean, 0.f);
    const float ra = __builtin_amdgcn_rsqf(var + 1e-5f), rb = -mean * ra;
    const int o = L_X + w * 7168 + (l >> 3) * 128 + (((l & 7) ^ (4 * (w & 1) + (l >> 4))) * 16);
#pragma unroll
    for (int i = 0; i < 7; i++) {
      float f[8];
#pragma unroll
      for (int j = 0; j < 4; j++) { f[2 * j] = bflo(xg[i][j]); f[2 * j + 1] = bfhi(xg[i][j]); }
#pragma unroll
      for (int j = 0; j < 8; j++) { f[j] = __builtin_fmaf(f[j], ra, rb); asm volatile("" : "+v"(f[j])); }
      const v4u xn = {pk2(f[0], f[1]), pk2(f[2], f[3]), pk2(f[4], f[5]), pk2(f[6], f[7])};
      lds_wr(sm, o + i * 1024, xn);
      if (i == 3) { SB(); DMA_KV(3); SB(); }
    }
  }
  *(unsigned*)(sm + L_MISC + (threadIdx.x == 0 ? 0 : 16)) = ep + 1u;
  SB();
  DMA_KV(4);
  BAR();
  STAMP(2);
  v4u xf[28];
  {
    const int row = 32 * tt + r, xrb = L_X + (row >> 3) * 7168 + (row & 7) * 128, sw = (row >> 1) & 7;
    int xa[4];
#pragma unroll
    for (int i = 0; i < 4; i++) xa[i] = xrb + (((2 * i + hh) ^ sw) * 16);
#pragma unroll
    for (int i = 0; i < 28; i++) xf[i] = lds_rd(sm, xa[i & 3] + (i >> 2) * 1024);
  }
  const unsigned tag = *(const unsigned*)(sm + L_MISC);
  const int trl = (c16 >> 2) * 112 + hd * 56 + min(4 * (g4 & 1) + (c16 & 3), 6) * 8;
  f16v acc = {};
  const int ba = fresh(L_KV + (w >> 2) * 896 + hh * 1792 + trl), bb2 = fresh(L_KV + (w >> 2) * 896 + hh * 1792 + trl + 57344);
#define RD_KV(i) tr_frag(sm, (i) < 16 ? ba + (i) * 3584 : bb2 + ((i) - 16) * 3584)
  v4u bb[4];
  {
    SB();
    {
      asm volatile("s_waitcnt vmcnt(8)" : "+v"(ut[0]), "+v"(ut[1]), "+v"(ut[2]), "+v"(ut[3]), "+v"(ut[4]), "+v"(ut[5]), "+v"(ut[6]), "+v"(ut[7]));
      const int d0 = w ? (b < 224 ? OFF_WC + (116 * idx + uks) * 1024 + (g4 & 1) * 59392 + hh * 512 + (b & 1) * 256 + c16 * 16 : OOB) : (l < 8 ? l16 + OFF_KVF + rh * 2048 + rj * 128 : OOB);
      const int d1 = w ? d0 + 29696 : ((ffn && l < 32) ? l16 + OFF_HS + ((tB * 8 + T) * 2 + a) * 512 : OOB);
      const unsigned f_ = w ? 0u : SENT;
      stc(v4u{lo2(ut[0], ut[1]), lo2(ut[2], ut[3]), lo2(ut[4], ut[5]), lo2(ut[6], ut[7])} | f_, ws, d0);
      stc(v4u{hi2(ut[0], ut[1]), hi2(ut[2], ut[3]), hi2(ut[4], ut[5]), hi2(ut[6], ut[7])} | f_, ws, d1);
      SB(); DMA_KV(5); SB();
      stc(sent, ws, pq_off + (w >> 2) * 1024);
      stc(sent, ws, ffn ? l16 + OFF_HF + ((4 * tB + a + 2 * (w >> 2)) * 28 + 4 * T + (w & 3)) * 1024 : OOB);
      SB(); DMA_KV(6);
    }
    SB(); asm volatile("s_waitcnt vmcnt(10) lgkmcnt(0)\n\ts_barrier" ::: "memory"); SB();
#pragma unroll
    for (int i = 0; i < 3; i++) bb[i] = RD_KV(i);
    sfor<0, 13>([&](auto II) {
      constexpr int i = decltype(II)::v;
      if constexpr (i % 7 == 0) __builtin_amdgcn_s_setprio(3 - i / 7);
      if constexpr (i == 4 || i == 8 || i == 12) DMA_Q(i / 4 - 1);
      bb[(i + 3) & 3] = RD_KV(i + 3);
      acc = mfma32(xf[i], bb[i & 3], acc);
      SB();
    });
    asm volatile("s_waitcnt vmcnt(3) lgkmcnt(0)\n\ts_barrier" ::: "memory"); SB();
    STAMP(3);
  }
  v4u wc[29];
  const int wc_off = OFF_WC + (8 * cC + w) * 29 * 1024, rq = ROTC ? (ai & 3) : 0;
#define LD_WC(i) wc[i] = ldf(ws, lo, wc_off + ((((i) / 7) + rq) & 3) * 7168, (i) % 7)
  const srd_t os = mks(p.wo, ffn ? D * D * 2 : 0);
  const int ov = (l >> 3) * 896 + (((l & 7) ^ (2 * ((l >> 4) & 3))) * 16) + 128 * T, og = 14 * (2 * (idx >> 4) + a);
#define DMA_WO(g) do { const int gg_ = wrap((g) + og, 56); dma_full(ov, os, gg_ * 7168, lbase + L_WO + gg_ * 1024); } while (0)
  if (w < 4) {
    __builtin_amdgcn_s_setprio(3);
    sfor<13, 28>([&](auto II) {
      constexpr int i = decltype(II)::v;
      if constexpr (i == 21) { SB(); asm volatile("s_barrier" ::: "memory"); SB(); }
      if constexpr (i + 3 < 28) bb[(i + 3) & 3] = RD_KV(i + 3);
      acc = mfma32(xf[i], bb[i & 3], acc);
      SB();
    });
    STAMP(4);
    {
      v4u fr[2];
#pragma unroll
      for (int j = 0; j < 8; j++) fr[j >> 2][j & 3] = pk2(phi(acc[2 * j]), phi(acc[2 * j + 1]));
      lds_wr(sm, L_KVIN + w * 2048 + l16, fr[0]); lds_wr(sm, L_KVIN + w * 2048 + 1024 + l16, fr[1]);
    }
    SB(); asm volatile("s_waitcnt vmcnt(0) lgkmcnt(0)\n\ts_barrier" ::: "memory"); SB();
    STAMP(5);
    {
      v4u ka[2], vb[2][2];
      const int ko = L_KVIN + hd * 4096 + g4 * 512 + c16 * 16;
#pragma unroll
      for (int t_ = 0; t_ < 2; t_++) { ka[t_] = lds_rd(sm, ko + t_ * 2048 + (w & 1) * 256); vb[t_][0] = lds_rd(sm, ko + 8192 + t_ * 2048); vb[t_][1] = lds_rd(sm, ko + 8192 + t_ * 2048 + 256); }
      SB();
      f4 kv[2] = {};
#pragma unroll
      for (int t_ = 0; t_ < 2; t_++) { kv[0] = mfma16(ka[t_], vb[t_][0], kv[0]); kv[1] = mfma16(ka[t_], vb[t_][1], kv[1]); }
      stc(tag4(kv[0], tag), ws, part_off); stc(tag4(kv[1], tag), ws, part_off + 1024);
    }
    STAMP(6);
    if (w == 0) {
      v4u pv[8];
      const int so = OFF_PART + rh * 131072 + ((rj >> 3) * 2 + ((rj >> 1) & 1)) * 1024 + ((rj >> 2) & 1) * 256 + (rj & 1) * 128;
      __builtin_amdgcn_s_sleep(DLY_KV);
#pragma nounroll
      for (int spin = 0; spin < SPIN_CAP; spin++) {
        MEMBAR();
        const int vo = fresh(g4 * 4096 + (c16 >> 3) * 512 + (c16 & 7) * 16);
        unsigned mm = 0;
#pragma unroll
        for (int i = 0; i < 8; i++) pv[i] = ldf(ws, vo, so + i * 16384, 0);
#pragma unroll
        for (int i = 0; i < 8; i++) mm = tagx(mm, pv[i], tag);
        if (__builtin_amdgcn_ballot_w64((mm & 0xffu) != 0u) == 0) break;
        DBG(3);
      }
      f4 s = {};
#pragma unroll
      for (int i = 0; i < 8; i++) s += __builtin_bit_cast(f4, pv[i] & 0xffffff00u);
#pragma unroll
      for (int i = 0; i < 4; i++) s[i] = sum32(sum16(s[i]));
      const v2u o = {min(pk2(s[0], s[1]), SENT - 1u), min(pk2(s[2], s[3]), SENT - 1u)};
      __builtin_amdgcn_raw_buffer_store_b64(o, ws, l < 16 ? (c16 & 7) * 16 + (c16 >> 3) * 8 : OOB, OFF_KVF + rh * 2048 + rj * 128, SC1);
      STAMP(9);
    } else {
      __builtin_amdgcn_s_setprio(0);
#pragma nounroll
      for (int g = 15 + w; g < 56; g += 3) DMA_WO(g);
      if (w == 3) {
        wait_flags(ws, OFF_FS + xcd * 8192, ~0ull, l, tag, 0 DBGP);
        if (b == 0 && l == 0) __builtin_amdgcn_raw_buffer_store_b32(tag, ws, 0, OFF_EPOCH, SC1);
      } else asm volatile("s_waitcnt vmcnt(0)" ::: "memory");
      lds_arrive(sm, L_MISC + 8, l);
    }
    __builtin_amdgcn_s_setprio(0);
  } else {
    __builtin_amdgcn_s_setprio(0);
    sfor<13, 28>([&](auto II) {
      constexpr int i = decltype(II)::v;
      if constexpr (i == 13) set_flag(ws, OFF_FS, b, w == 7, l, tag);
      if constexpr (i == 21) { SB(); asm volatile("s_barrier" ::: "memory"); SB(); }
      if constexpr (i == 15 || i == 18 || i == 22 || i == 25) {
        constexpr int c = i == 15 ? 3 : (i == 18 ? 4 : (i == 22 ? 5 : 6));
        dma_q(wv, wsrd, (wrap(c + rot, 7) * 8 + w - 4) * 21504, lbase + L_X + (c * 8 + w - 4) * 896); DMA_Q(c);
      }
      if constexpr (i + 3 < 28) bb[(i + 3) & 3] = RD_KV(i + 3);
      acc = mfma32(xf[i], bb[i & 3], acc);
      SB();
    });
    STAMP(4);
    {
      const unsigned pad = r == 28 ? ONES : 0u;
      v4u fr[2];
#pragma unroll
      for (int j = 0; j < 8; j++) { const unsigned t_ = pk2(acc[2 * j], acc[2 * j + 1]); fr[j >> 2][j & 3] = r < 28 ? t_ : pad; }
      lds_wr(sm, L_KVIN + w * 2048 + l16, fr[0]); lds_wr(sm, L_KVIN + w * 2048 + 1024 + l16, fr[1]);
    }
    SB();
#pragma unroll
    for (int i = 0; i < 4; i++) DMA_WO(4 * i + w - 4);
    SB(); asm volatile("s_waitcnt vmcnt(4) lgkmcnt(0)\n\ts_barrier" ::: "memory"); SB();
    STAMP(5);
    const int qa = fresh(L_X + hh * 896 + trl);
    f16v aq = {};
#pragma unroll
    for (int i = 0; i < 3; i++) bb[i] = tr_frag(sm, qa + i * 1792);
    SB();
    sfor<0, 28>([&](auto II) {
      constexpr int i = decltype(II)::v;
      if constexpr (i + 3 < 28) bb[(i + 3) & 3] = tr_frag(sm, qa + (i + 3) * 1792);
      aq = mfma32(bb[i & 3], xf[i], aq);
      SB();
    });
    __builtin_amdgcn_s_setprio(3);
#pragma unroll
    for (int s = 0; s < 2; s++) {
      v4u pf = {pk2(phi(aq[8 * s]), phi(aq[8 * s + 1])), pk2(phi(aq[8 * s + 2]), phi(aq[8 * s + 3])), pk2(phi(aq[8 * s + 4]), phi(aq[8 * s + 5])), pk2(phi(aq[8 * s + 6]), phi(aq[8 * s + 7]))};
      if (s == 1) { pf[2] = hh ? 0u : pf[2]; pf[3] = hh ? 0u : pf[3]; }
      stc(canon(pf), ws, pq_off + s * 1024);
    }
    __builtin_amdgcn_s_setprio(0);
    STAMP(8);
    asm volatile("s_waitcnt vmcnt(2)" ::: "memory");
    lds_arrive(sm, L_MISC + 8, l);
  }
  lds_wait(lbase + L_MISC + 8, 7u);
  SB();
  STAMP(10);
  if (!ffn) {
    if (jcs < 28) {
      if (w < 7) {
        const rsrc_t ds = mk(p.wd, F * D * 2);
        unsigned v[4][8];
        const int dv = fresh(hh * 7168 + r * 4), so = 64 * jcs * 896 + 128 * w;
#pragma unroll
        for (int u = 0; u < 4; u++)
#pragma unroll
          for (int j = 0; j < 8; j++) v[u][j] = __builtin_amdgcn_raw_buffer_load_b32(ds, dv + (u & 1) * 896, so + (u >> 1) * 28672 + (j & 3) * 1792 + (j >> 2) * 14336, 0);
        SB();
#pragma unroll
        for (int u = 0; u < 4; u++) {
          const int d0 = OFF_WD + (224 * w + 4 * jcs + u) * 1024 + l16;
          stc(v4u{lo2(v[u][0], v[u][1]), lo2(v[u][2], v[u][3]), lo2(v[u][4], v[u][5]), lo2(v[u][6], v[u][7])}, ws, d0);
          stc(v4u{hi2(v[u][0], v[u][1]), hi2(v[u][2], v[u][3]), hi2(v[u][4], v[u][5]), hi2(v[u][6], v[u][7])}, ws, d0 + 114688);
        }
      }
      const int ft = 4 * jcs + (w >> 1), lo = fresh(l16);
      v4u v[14]; float cs = 0.f;
#pragma unroll
      for (int i = 0; i < 14; i++) v[i] = ldf(ws, lo, OFF_WC + (ft * 29 + 14 * (w & 1)) * 1024, i);
#pragma unroll
      for (int i = 0; i < 14; i++)
#pragma unroll
        for (int j = 0; j < 4; j++) cs = dot2(v[i][j], ONES, cs);
      cs = sum32(cs);
      *(float*)(sm + L_HST + w * 256 + l * 4) = cs;
      BAR();
      if (!(w & 1)) {
        cs += *(const float*)(sm + L_HST + (w + 1) * 256 + l * 4);
        const unsigned hi = pk2(cs, cs), lw = pk2(cs - bflo(hi), 0.f);
        const v4u fr = {l < 32 ? hi : 0u, l < 32 ? lw : 0u, 0u, 0u};
        stc(fr, ws, l16 + OFF_WC + (ft * 29 + 28) * 1024);
      }
      asm volatile("s_waitcnt vmcnt(0)" ::: "memory");
      BAR();
    }
    if (w == 0) set_flag(ws, OFF_FD, b, true, l, tag);
  } else {
    const int c4 = w & 3, th = w >> 2;
    v4u wo[16], pq[8], kvf[4], hv[8];
    int woa[2];
#pragma unroll
    for (int e = 0; e < 2; e++) woa[e] = L_WO + (16 * (g4 >> 1) + 4 * (g4 & 1) + (c16 >> 2)) * 128 + (((2 * c4 + ((c16 >> 1) & 1)) ^ (2 * ((((e + g4) & 1) << 1) + (c16 >> 3)))) * 16) + (c16 & 1) * 8;
    const int wo8 = g4 == 3 ? 0 : 1024;
    const int hoff = OFF_HF + (4 * tC * 28 + 4 * w) * 1024;
    v2u xr[2];
    {
      const int lo = fresh(l16);
#pragma unroll
      for (int i = 0; i < NPF0; i++) LD_WC(i);
      SB();
    }
    if (w != 0 && DLY_PQ) __builtin_amdgcn_s_sleep(DLY_PQ);
    __builtin_amdgcn_s_setprio(2);
    poll8(pq, ws, l16, OFF_PQ + ((4 * tB + a) * 32 + 4 * w) * 1024, OFF_PQ + ((4 * tB + a + 2) * 32 + 4 * w) * 1024, 4 DBGP, [&] {
      const int lo = fresh(l16);
      __builtin_amdgcn_s_setprio(0);
#pragma unroll
      for (int ps = 0; ps < 2; ps++) xr[ps] = *(const v2u*)((const char*)p.x + (size_t)((4 * tB + a + 2 * ps) * 32 + 16 * th + c16) * 896 + (64 * T + 16 * c4 + 4 * g4) * 2);
#pragma unroll
      for (int i = NPF0; i < NPF1; i++) LD_WC(i);
      SB();
#pragma unroll
      for (int i = 0; i < 16; i++) {
        const v2u x0 = tr_half(sm, woa[i & 1] + i * 3584), x1 = tr_half(sm, woa[i & 1] + wo8 + i * 3584);
        wo[i] = v4u{x0[0], x0[1], x1[0], x1[1]};
      }
    });
    STAMP(7);
    if (w != 0 && DLY_KF) __builtin_amdgcn_s_sleep(DLY_KF);
    __builtin_amdgcn_s_setprio(3);
    poll_frags<4>(kvf, ws, l16, OFF_KVF + 4 * w * 1024, 5 DBGP);
    __builtin_amdgcn_s_setprio(0);
    STAMP(11);
    sfor<0, 2>([&](auto PS) {
      constexpr int ps = decltype(PS)::v;
      const int lo = fresh(l16);
      f16v n[2] = {};
#pragma unroll
      for (int s = 0; s < 2; s++)
#pragma unroll
        for (int hd = 0; hd < 2; hd++) n[hd] = mfma32(kvf[2 * hd + s], pq[4 * ps + 2 * hd + s], n[hd]);
      constexpr int pa = ps ? NPF1 + 4 : NPF1;
      LD_WC(pa); LD_WC(pa + 1);
      SB();
#pragma unroll
      for (int hd = 0; hd < 2; hd++) {
        const auto sw = __builtin_amdgcn_permlane32_swap(__float_as_uint(n[hd][12]), __float_as_uint(n[hd][12]), false, false);
        const float inv = __builtin_amdgcn_rcpf(__uint_as_float(sw[1]) + 1e-6f);
        v4u o0, o1;
#pragma unroll
        for (int i = 0; i < 4; i++) { o0[i] = pk2(n[hd][2 * i] * inv, n[hd][2 * i + 1] * inv); o1[i] = pk2(n[hd][8 + 2 * i] * inv, n[hd][9 + 2 * i] * inv); }
        o1[2] = hh ? 0u : o1[2];
        lds_wr(sm, (ps ? L_OB1 : L_OB0) + (4 * w + 2 * hd) * 1024 + lo, o0);
        lds_wr(sm, (ps ? L_OB1 : L_OB0) + (4 * w + 2 * hd + 1) * 1024 + lo, o1);
      }
      BAR();
      if (ps == 0) STAMP(12);
      v4u ob[8];
      const int oo = fresh((ps ? L_OB1 : L_OB0) + g4 * 512 + th * 256 + c16 * 16), vh = fresh(w < 7 ? l16 : OOB);
#pragma unroll
      for (int i = 0; i < 8; i++) ob[i] = lds_rd(sm, oo + i * 2048);
      f4 hacc = {bflo(xr[ps][0]), bfhi(xr[ps][0]), bflo(xr[ps][1]), bfhi(xr[ps][1])};
      SB();
#pragma unroll
      for (int i = 0; i < 16; i++) {
        hacc = mfma16(wo[i], ob[i & 7], hacc);
        if (i < 8) ob[i] = lds_rd(sm, oo + (8 + i) * 2048);
        if (ps == 1 && (i & 1)) hv[i >> 1] = ldf(ws, vh, hoff + (i >> 3) * 28 * 1024, (i >> 1) & 3);
        SB();
      }
      const unsigned p0 = min(pk2(hacc[0], hacc[1]), SENT - 1u), p1 = min(pk2(hacc[2], hacc[3]), SENT - 1u);
      const auto s0 = __builtin_amdgcn_permlane16_swap(p0, p0, false, false), s1 = __builtin_amdgcn_permlane16_swap(p1, p1, false, false);
      const v4u hf = {s0[0], s1[0], s0[1], s1[1]};
      stc(hf, ws, (g4 & 1) ? OOB : OFF_HF + ((4 * tB + a + 2 * ps) * 28 + 4 * T + c4) * 1024 + (g4 >> 1) * 512 + th * 256 + c16 * 16);
      STAMP(13 + ps);
      LD_WC(pa + 2); LD_WC(pa + 3);
      {
        const float s_ = dot2(p0, ONES, dot2(p1, ONES, 0.f)), ss_ = dot2(p0, p0, dot2(p1, p1, 0.f));
        *(f2*)(sm + L_XN + ((c4 * 4 + g4) * 32 + 16 * th + c16) * 16 + ps * 8) = f2{s_, ss_};
      }
      SB();
    });
    v4u wd[28], av[14];
    const int rd = ROTD ? 2 * wrap(wrap(T + 2 * (2 * (idx >> 4) + a), 7), 7) : 0;
    const int wd_off = OFF_WD + ((2 * T) * 112 + 14 * w) * 1024;
#define LD_WD(i) wd[i] = ldf(ws, (ABL & 1) ? OOB : lo, wd_off + (((i) & 1) * 112 + wrap((((i) >> 2) << 1) + rd, 14)) * 1024, ((i) >> 1) & 1)
#define LD_AV(i, off) av[i] = ldf(ws, (ABL & 4) ? OOB : lo, (off) + wrap(((i) & ~1) + rd, 14) * 1024, (i) & 1)
    const int g2a = w < 7 ? hoff + 56 * 1024 : OFF_HS + tC * 8192, g2b = w < 7 ? hoff + 84 * 1024 : OFF_HS + tC * 8192 + 3 * 1024;
    int hw[4];
#pragma unroll
    for (int i = 0; i < 4; i++) hw[i] = w < 7 ? wrap(4 * w + i + 28 - 7 * rq, 28) * 1024 : L_HST - L_H1 + i * 1024;
    const int hk = w < 7 ? 28 * 1024 : 3 * 1024;
    float c1[4], vv[4]; unsigned mneg[4];
    f4 sq[7];
    auto stats_read = [&] {
#pragma unroll
      for (int i = 0; i < 7; i++) sq[i] = *(const f4*)(sm + L_HST + i * 1024 + l16);
    };
    auto stats_math = [&](int ps) {
      float s_ = sq[0][2 * ps], ss_ = sq[0][2 * ps + 1];
#pragma unroll
      for (int i = 1; i < 7; i++) { s_ += sq[i][2 * ps]; PIN(s_); ss_ += sq[i][2 * ps + 1]; }
      const float nm = s_ * (-1.f / D), ve = fmaxf(ss_ * (1.f / D) - nm * nm, 0.f) + 1e-5f;
      const float cc = __builtin_amdgcn_rsqf(ve) * -1.44269504089f;
      const unsigned mh = pk2(nm, 0.f), mm = pk2(nm, nm - bflo(mh));
      const auto s1 = __builtin_amdgcn_permlane32_swap(__float_as_uint(cc), __float_as_uint(cc), false, false);
      const auto s2 = __builtin_amdgcn_permlane32_swap(__float_as_uint(ve), __float_as_uint(ve), false, false);
      const auto s3 = __builtin_amdgcn_permlane32_swap(mm, mm, false, false);
#pragma unroll
      for (int k = 0; k < 2; k++) { c1[2 * ps + k] = __uint_as_float(s1[k]); vv[2 * ps + k] = __uint_as_float(s2[k]); mneg[2 * ps + k] = s3[k]; }
    };
    BAR();
    asm volatile("" : "+v"(hv[0]), "+v"(hv[1]), "+v"(hv[2]), "+v"(hv[3]), "+v"(hv[4]), "+v"(hv[5]), "+v"(hv[6]), "+v"(hv[7]));
    if (w < 7) {
      unsigned mm = 0;
#pragma unroll
      for (int i = 0; i < 8; i++) mm = mx4(mm, hv[i]);
      if (bad(mm)) { DBG(6); poll8(hv, ws, l16, hoff, hoff + 28 * 1024, 6 DBGP, [] {}); }
      STAMP(15);
#pragma unroll
      for (int i = 0; i < 8; i++) lds_wr(sm, L_H0 + l16 + hw[i & 3] + (i >> 2) * 28 * 1024, hv[i]);
    } else {
      f4 t = {};
#pragma unroll
      for (int i = 0; i < 8; i++) t += *(const f4*)(sm + L_XN + i * 1024 + l16);
#pragma unroll
      for (int i = 0; i < 4; i++) t[i] = sum32(t[i]);
      stc(canon(__builtin_bit_cast(v4u, t)), ws, l < 32 ? l16 + OFF_HS + ((tB * 8 + T) * 2 + a) * 512 : OOB);
    }
    BAR();
    STAMP(16);
    const int ftt = 4 * tB + a + 2 * (w >> 2);
    v4u res;
    const int a0off = OFF_ACT + ((4 * tB + a) * 112 + 14 * w) * 1024;
    const srd_t srd = mks(p.ws, WS_BYTES);
    const int fdo = (28 + (g4 & 1) * 32 + (c16 >> 2)) * 128 + (l & 3) * 4;
    f16v ac[2][2] = {};
    float eo[2]; v4u af;
    auto epi = [&](auto PS, auto EE) {
      constexpr int ps = decltype(PS)::v, e = decltype(EE)::v, k = e >> 3, j = e & 7;
      float g = ac[ps][k][j], t = g * c1[2 * ps + k]; PIN(t);
      float gu = g * ac[ps][k][8 + j]; PIN(gu);
      eo[j & 1] = gu * __builtin_amdgcn_rcpf(__builtin_fmaf(__builtin_amdgcn_exp2f(t), vv[2 * ps + k], vv[2 * ps + k]));
      PIN(eo[j & 1]);
      if constexpr (j & 1) af[j >> 1] = min(pk2(eo[0], eo[1]), SENT - 1u);
      if constexpr (j == 7) stc(af, ws, l16 + OFF_ACT + ((4 * tC + 2 * ps + k) * 112 + 8 * cC + w) * 1024);
    };
    auto fold_mean = [&](int ps) {
#pragma unroll
      for (int k = 0; k < 2; k++) { const v4u bx = {mneg[2 * ps + k], mneg[2 * ps + k] & 0xffffu, 0u, 0u}; ac[ps][k] = mfma32(wc[28], bx, ac[ps][k]); }
    };
    sfor<0, 2>([&](auto PS) {
      constexpr int ps = decltype(PS)::v;
      const int lo = fresh(l16);
      const int hb = fresh((ps ? L_H1 : L_H0) + l16);
      v4u bb[3][2];
      unsigned fa = 0, mm = 0;
#pragma unroll
      for (int i = 0; i < 2; i++) { bb[i][0] = lds_rd(sm, hb + i * 1024); bb[i][1] = lds_rd(sm, hb + (28 + i) * 1024); }
      if constexpr (ps == 1) { fold_mean(0); fold_mean(1); }
      SB();
      sfor<0, 28>([&](auto II) {
        constexpr int i = decltype(II)::v;
        if constexpr (i % 7 == 0) __builtin_amdgcn_s_setprio(3 - i / 7);
        if constexpr (i % 4 == 0) STAMPX(1 + ps, 1 + i / 4);
        sfor<0, 2>([&](auto KK) {
          constexpr int k = decltype(KK)::v;
          if constexpr (i + 2 < 28) bb[(i + 2) % 3][k] = lds_rd(sm, hb + (k * 28 + i + 2) * 1024);
          ac[ps][k] = mfma32(wc[i], bb[i % 3][k], ac[ps][k]);
          if constexpr (ps == 0 && k == 0 && i >= 15 && i < 23) {
            constexpr int f = i - 15;
            mm = mx4(mm, hv[f]);
            lds_wr(sm, L_H1 + lo + hw[f & 3] + (f >> 2) * hk, hv[f]);
          }
          if constexpr (ps == 1 && i >= 1 && i < 9) epi(IC<0>{}, IC<2 * (i - 1) + k>{});
          SB();
        });
        if constexpr (ps == 0) {
          if constexpr (i < 6 && NPF1 + 8 + 2 * i < 28) { LD_WC(NPF1 + 8 + 2 * i); LD_WC(NPF1 + 9 + 2 * i); }
          if constexpr (i >= 6 && i < 14) hv[i - 6] = ldf(ws, lo, i < 10 ? g2a : g2b, (i - 6) & 3);
          if constexpr (i == 5) fa = __builtin_amdgcn_raw_buffer_load_b32(ws, fdo, OFF_FD + xcd * 8192, SC1);
          if constexpr (i == 14) { if (__builtin_amdgcn_ballot_w64(fa != tag) != 0) DBG(2); wait_words(fa, tag, srd, fdo, OFF_FD + xcd * 8192); wc[28] = ldf(ws, lo, wc_off + 28 * 1024, 0); }
          if constexpr (i == 16 || i == 18) stc(sent, ws, lo + OFF_ACT + ((4 * tC + i / 2 - 6) * 112 + 8 * cC + w) * 1024);
          if constexpr (i >= 28 - NWD0) LD_WD(i - (28 - NWD0));
          if constexpr (i == 22) *(unsigned*)(sm + L_MISC + (mm == SENT ? 12 : 16)) = 1u;
          if constexpr (i == 23) { SB(); asm volatile("s_waitcnt lgkmcnt(2)\n\ts_barrier" ::: "memory"); SB(); stats_read(); fa = *(const unsigned*)(sm + L_MISC + 12); }
          if constexpr (i == 25) stats_math(0);
          if constexpr (i == 26) stats_math(1);
          SB();
        } else {
          if constexpr (NWD0 + i < 28) LD_WD(NWD0 + i);
          if constexpr (i == 15) {
            SB(); asm volatile("s_waitcnt vmcnt(8)\n\ts_barrier" ::: "memory"); SB();
            __builtin_amdgcn_raw_buffer_store_b32(tag, ws, threadIdx.x == 0 ? 0 : OOB, OFF_FA + (tC * 16 + cC) * 128, SC1);
          }
          if constexpr (i == 23) res = ldf(ws, lo, OFF_HF + (ftt * 28 + 4 * T + (w & 3)) * 1024, 0);
          if constexpr (i == 22) fa = __builtin_amdgcn_raw_buffer_load_b32(ws, l < 14 ? l * 128 : 0, OFF_FA + tB * 2048, SC1);
          if constexpr (i == 27) {
            if (__builtin_amdgcn_ballot_w64(fa != tag) != 0) DBG(10);
            wait_words(fa, tag, srd, l < 14 ? l * 128 : 0, OFF_FA + tB * 2048);
          }
          SB();
        }
      });
      if constexpr (ps == 0) {
        if (__builtin_amdgcn_readfirstlane(fa) != 0u) {
          DBG(7);
          BAR();
          poll8(hv, ws, l16, g2a, g2b, 8 DBGP, [] {});
#pragma unroll
          for (int f = 0; f < 8; f++) lds_wr(sm, L_H1 + l16 + hw[f & 3] + (f >> 2) * hk, hv[f]);
          BAR();
          stats_read(); stats_math(0); stats_math(1);
        }
      }
      STAMP(17 + 2 * ps);
    });
    {
      int lo = fresh(l16);
      v4u av2[14];
      sfor<0, 16>([&](auto EE) {
        constexpr int e = decltype(EE)::v;
        if constexpr (e < 14) LD_AV(e, a0off);
        epi(IC<1>{}, EE);
        SB();
      });
      STAMP(20);
      const rsrc_t os = mk(p.out, S * D * 2);
      const int oo = (ftt * 32 + r) * 896 + (64 * T + 16 * (w & 3) + 8 * (l >> 5)) * 2;
#define LD_AV2(i) av2[i] = ldf(ws, lo, a0off + 2 * 112 * 1024 + wrap(((i) & ~1) + rd, 14) * 1024, (i) & 1)
      {
        f16v acc0 = {}, acc1 = {};
        asm volatile("s_barrier" ::: "memory");
        SB();
        sfor<0, 14>([&](auto II) {
          constexpr int i = decltype(II)::v;
          if constexpr (i % 4 == 0) __builtin_amdgcn_s_setprio(3 - i / 4);
          acc0 = mfma32(wd[2 * i], av[i], acc0); acc1 = mfma32(wd[2 * i + 1], av[i], acc1);
          if constexpr (i >= AV2AT) { LD_AV2(i - AV2AT); }
          SB();
        });
        STAMP(21);
        sfor<0, 8>([&](auto QQ) {
          constexpr int q = decltype(QQ)::v;
          if constexpr (14 - AV2AT + q < 14) { LD_AV2(14 - AV2AT + q); }
          const f16v& a_ = q < 4 ? acc0 : acc1;
          lds_wr(sm, L_H0 + w * 8192 + l16 + q * 1024, f4u(a_[4 * (q & 3)], a_[4 * (q & 3) + 1], a_[4 * (q & 3) + 2], a_[4 * (q & 3) + 3]));
          SB();
        });
#pragma unroll
        for (int i = 22 - AV2AT; i < 14; i++) LD_AV2(i);
        SB();
      }
      {
        f16v acc0 = {}, acc1 = {};
        sfor<0, 14>([&](auto II) {
          constexpr int i = decltype(II)::v;
          if constexpr (i % 4 == 0) __builtin_amdgcn_s_setprio(3 - i / 4);
          acc0 = mfma32(wd[2 * i], av2[i], acc0); acc1 = mfma32(wd[2 * i + 1], av2[i], acc1);
          SB();
        });
        if (__builtin_amdgcn_ballot_w64(acc0[0] != acc0[0]) != 0) {
          DBG(11);
#pragma nounroll
          for (int spin = 0; spin < SPIN_CAP; spin++) {
            MEMBAR();
            unsigned mm = 0;
            lo = fresh(l16);
#pragma unroll
            for (int i = 0; i < 14; i++) LD_AV2(i);
#pragma unroll
            for (int i = 0; i < 14; i++) mm = mx4(mm, av2[i]);
            if (!bad(mm)) break;
          }
          acc0 = f16v{}; acc1 = f16v{};
#pragma unroll
          for (int i = 0; i < 14; i++) { acc0 = mfma32(wd[2 * i], av2[i], acc0); acc1 = mfma32(wd[2 * i + 1], av2[i], acc1); }
        }
        STAMP(22);
        red_write(sm, L_H1 + w * 8192 + l16, acc0, acc1);
        BAR();
        PIN(res);
        __builtin_amdgcn_raw_buffer_store_b128(red_final(sm, fresh((w < 4 ? L_H0 : L_H1) + (w & 3) * 1024 + l16), res), os, oo, 0, 0);
      }
    }
  }
  asm volatile(".Lfb_end:");
  STAMP(23);
}

extern "C" void kernel_launch(void* const* d_in, const int* in_sizes, int n_in, void* d_out, int out_size, void* d_ws, size_t ws_size, hipStream_t stream) {
  Ptrs p;
  p.x = d_in[0]; p.wqkv = d_in[1]; p.wo = d_in[2]; p.wg = d_in[4]; p.wu = d_in[5]; p.wd = d_in[6]; p.out = (char*)d_out; p.ws = (char*)d_ws;
  fused_block<<<256, 512, 0, stream>>>(p);
}
```

```cpp
#include <hip/hip_runtime.h>
#include <stdint.h>

constexpr int S = 2048, D = 448, F = 1792;
typedef unsigned v4u __attribute__((ext_vector_type(4)));
typedef unsigned v2u __attribute__((ext_vector_type(2)));
typedef float f4 __attribute__((ext_vector_type(4)));
typedef float f2 __attribute__((ext_vector_type(2)));
typedef float f16v __attribute__((ext_vector_type(16)));
typedef __bf16 bf2 __attribute__((ext_vector_type(2)));
typedef __bf16 bf8 __attribute__((ext_vector_type(8)));
typedef int srd_t __attribute__((ext_vector_type(4)));
typedef __amdgpu_buffer_rsrc_t rsrc_t;
#define DI __device__ __forceinline__
#define SB() __builtin_amdgcn_sched_barrier(0)
#define MEMBAR() asm volatile("" ::: "memory")
#define BAR() do { SB(); asm volatile("s_waitcnt lgkmcnt(0)\n\ts_barrier" ::: "memory"); SB(); } while (0)

constexpr int OFF_EPOCH = 0, OFF_FA = 32768;
constexpr int OFF_FS = 65536, OFF_FC = OFF_FS + 65536, OFF_FD = OFF_FC + 65536;
constexpr int OFF_WC = 262144;
constexpr int OFF_WD = OFF_WC + 112 * 29 * 1024;
constexpr int OFF_PART = OFF_WD + 1568 * 1024;
constexpr int OFF_KVF = OFF_PART + 2048 * 1024;
constexpr int OFF_PQ = OFF_KVF + 32 * 1024;
constexpr int OFF_HF = OFF_PQ + 2048 * 1024;
constexpr int OFF_HS = OFF_HF + 1792 * 1024;
constexpr int OFF_ACT = OFF_HS + 128 * 1024;
constexpr int WS_BYTES = OFF_ACT + 7168 * 1024;
constexpr int OOB = 0x40000000;
constexpr int SC1 = 16;
constexpr unsigned SENT = 0xFFFFFFFFu;
constexpr int SPIN_CAP = 1 << 16;

#define STAMP(i)
#define STAMPX(set, i)
#define DBG(i)
#define DBGA
#define DBGP

DI rsrc_t mk(const void* p, int bytes) { return __builtin_amdgcn_make_buffer_rsrc((void*)p, 0, bytes, 0x00020000); }
DI v4u ldf(rsrc_t r, int voff, int base, int i) { return __builtin_amdgcn_raw_buffer_load_b128(r, voff + (i & 3) * 1024, base + (i >> 2) * 4096, SC1); }
DI void stc(v4u v, rsrc_t r, int off) { __builtin_amdgcn_raw_buffer_store_b128(v, r, off, 0, SC1); }
DI v4u canon(v4u v) { v4u o; for (int i = 0; i < 4; i++) o[i] = min(v[i], SENT - 1u); return o; }
DI f16v mfma32(v4u a, v4u b, f16v c) { return __builtin_amdgcn_mfma_f32_32x32x16_bf16(__builtin_bit_cast(bf8, a), __builtin_bit_cast(bf8, b), c, 0, 0, 0); }
DI f4 mfma16(v4u a, v4u b, f4 c) { return __builtin_amdgcn_mfma_f32_16x16x32_bf16(__builtin_bit_cast(bf8, a), __builtin_bit_cast(bf8, b), c, 0, 0, 0); }
DI unsigned pk2(float lo, float hi) { f2 v = {lo, hi}; return __builtin_bit_cast(unsigned, __builtin_convertvector(v, bf2)); }
DI float dot2(unsigned a, unsigned b, float c) { return __builtin_amdgcn_fdot2_f32_bf16(__builtin_bit_cast(bf2, a), __builtin_bit_cast(bf2, b), c, false); }
DI float bflo(unsigned u) { return __uint_as_float(u << 16); }
DI float bfhi(unsigned u) { return __uint_as_float(u & 0xffff0000u); }
DI float phi(float t) { return (fmaxf(t, 0.f) + 1e-6f) + __builtin_amdgcn_fmed3f(__builtin_amdgcn_exp2f(t * 1.44269504089f), 0.f, 1.f); }
constexpr unsigned ONES = 0x3f803f80u;
DI v4u lds_rd(const char* sm, int off) { return *(const v4u*)(sm + off); }
DI void lds_wr(char* sm, int off, v4u v) { *(v4u*)(sm + off) = v; }
DI v4u f4u(float a, float b, float c, float d) { v4u r = {__float_as_uint(a), __float_as_uint(b), __float_as_uint(c), __float_as_uint(d)}; return r; }
DI unsigned mx4(unsigned m, v4u v) { return max(max(max(m, v[0]), v[1]), max(v[2], v[3])); }
DI bool bad(unsigned m) { return __builtin_amdgcn_ballot_w64(m == SENT) != 0; }
DI float sum16(float v) { auto s = __builtin_amdgcn_permlane16_swap(__float_as_uint(v), __float_as_uint(v), false, false); return __uint_as_float(s[0]) + __uint_as_float(s[1]); }
DI float sum32(float v) { auto s = __builtin_amdgcn_permlane32_swap(__float_as_uint(v), __float_as_uint(v), false, false); return __uint_as_float(s[0]) + __uint_as_float(s[1]); }
template <int CTRL> DI float dpp_add(float v) { return v + __builtin_bit_cast(float, __builtin_amdgcn_update_dpp(0, __builtin_bit_cast(int, v), CTRL, 0xf, 0xf, true)); }
DI float wave_sum(float v) { v = dpp_add<0xB1>(v); v = dpp_add<0x4E>(v); v = dpp_add<0x141>(v); v = dpp_add<0x140>(v); return sum32(sum16(v)); }
DI int fresh(int v) { asm volatile("" : "+v"(v)); return v; }
#define PIN(x) asm volatile("" : "+v"(x))
DI int sfresh(int v) { asm volatile("" : "+s"(v)); return v; }
DI int wrap(int v, int n) { return v >= n ? v - n : v; }
template <int I> struct IC { static constexpr int v = I; };
template <int I, int N, class Fn> DI void sfor(Fn&& f) { if constexpr (I < N) { f(IC<I>{}); sfor<I + 1, N>(f); } }

struct Ptrs { const void *x, *wqkv, *wo, *wg, *wu, *wd; char* out; char* ws; };

DI srd_t mks(const void* p, int bytes) { return srd_t{(int)(size_t)p, (int)((size_t)p >> 32) & 0xffff, bytes, 0x00020000}; }
DI v4u tag4(f4 v, unsigned tag) { v4u o; for (int i = 0; i < 4; i++) o[i] = __builtin_amdgcn_perm(__float_as_uint(v[i]), tag, 0x07060500u); return o; }
DI unsigned tagx(unsigned m, v4u v, unsigned tag) { return m | ((v[0] ^ tag) | (v[1] ^ tag)) | ((v[2] ^ tag) | (v[3] ^ tag)); }
DI unsigned lo2(unsigned a, unsigned b) { return __builtin_amdgcn_perm(b, a, 0x05040100u); }
DI unsigned hi2(unsigned a, unsigned b) { return __builtin_amdgcn_perm(b, a, 0x07060302u); }
DI void wait_flags(rsrc_t ws, int off, unsigned long long lines, int l, unsigned tag, int id DBGA) {
  const int vo = ((lines >> l) & 1) ? l * 128 : OOB;
#pragma nounroll
  for (int spin = 0; spin < SPIN_CAP; spin++) {
    MEMBAR();
    const v4u f = ldf(ws, vo, off, 0);
    if ((__builtin_amdgcn_ballot_w64(((f[0] ^ tag) | (f[1] ^ tag) | (f[2] ^ tag) | (f[3] ^ tag)) != 0u) & lines) == 0) break;
    DBG(id);
    __builtin_amdgcn_s_sleep(2);
  }
}
DI void set_flag(rsrc_t ws, int off, int b, bool on, int l, unsigned tag) {
  __builtin_amdgcn_raw_buffer_store_b32(tag, ws, (on && l < 8) ? l * 8192 : OOB, off + (b >> 2) * 128 + (b & 3) * 4, SC1);
}
DI void wait_words(unsigned v, unsigned tag, srd_t srd, int voff, int soff) {
  int cnt;
  asm volatile("s_mov_b32 %1, 0x10000\n"
    "1:\n\tv_cmp_ne_u32 vcc, %0, %2\n\ts_cbranch_vccz 2f\n\t"
    "buffer_load_dword %0, %3, %4, %5 offen sc1\n\ts_waitcnt vmcnt(0)\n\t"
    "s_sub_u32 %1, %1, 1\n\ts_cbranch_scc0 1b\n"
    "2:" : "+v"(v), "=&s"(cnt) : "v"(tag), "v"(voff), "s"(srd), "s"(soff) : "vcc", "scc", "memory");
}
template <int N> DI void poll_frags(v4u (&v)[N], rsrc_t ws, int voff, int base, int id DBGA) {
#pragma nounroll
  for (int spin = 0; spin < SPIN_CAP; spin++) {
    MEMBAR();
    const int lo = fresh(voff);
    unsigned m = 0;
#pragma unroll
    for (int i = 0; i < N; i++) v[i] = ldf(ws, lo, base, i);
#pragma unroll
    for (int i = 0; i < N; i++) m = mx4(m, v[i]);
    if (!bad(m)) break;
    DBG(id);
  }
}
template <class Fn> DI void poll8(v4u (&v)[8], rsrc_t ws, int voff, int s0, int s1, int id DBGA, Fn&& hook) {
  {
    const int lo = fresh(voff);
    unsigned m = 0;
#pragma unroll
    for (int i = 0; i < 8; i++) v[i] = ldf(ws, lo, i < 4 ? s0 : s1, i & 3);
    SB(); hook(); SB();
#pragma unroll
    for (int i = 0; i < 8; i++) m = mx4(m, v[i]);
    if (!bad(m)) return;
    DBG(id);
  }
#pragma nounroll
  for (int spin = 0; spin < SPIN_CAP; spin++) {
    MEMBAR();
    const int lo = fresh(voff);
    unsigned m = 0;
#pragma unroll
    for (int i = 0; i < 8; i++) v[i] = ldf(ws, lo, i < 4 ? s0 : s1, i & 3);
#pragma unroll
    for (int i = 0; i < 8; i++) m = mx4(m, v[i]);
    if (!bad(m)) break;
    DBG(id);
  }
}
typedef short s4v __attribute__((ext_vector_type(4)));
#define LAS __attribute__((address_space(3)))
DI v4u tr_frag(const char* sm, int off) {
  const v2u a = __builtin_bit_cast(v2u, __builtin_amdgcn_ds_read_tr16_b64_v4i16((LAS s4v*)((LAS char*)sm + off)));
  const v2u b = __builtin_bit_cast(v2u, __builtin_amdgcn_ds_read_tr16_b64_v4i16((LAS s4v*)((LAS char*)sm + off + 448)));
  return v4u{a[0], a[1], b[0], b[1]};
}
DI void dma_kv(int voff, srd_t srd, int soff, int lds) {
  unsigned keep;
  asm volatile("s_mov_b32 %0, m0\n\ts_mov_b32 exec_hi, 0x00ffffff\n\ts_mov_b32 m0, %4\n\ts_nop 0\n\t"
               "buffer_load_dwordx4 %1, %2, %3 offen offset:896 lds\n\tbuffer_load_dwordx4 %1, %2, %3 offen offset:1792 lds\n\t"
               "s_mov_b32 m0, %0\n\ts_mov_b32 exec_hi, -1" : "=&s"(keep) : "v"(voff), "s"(srd), "s"(soff), "s"(lds - 896) : "memory");
}
DI void dma_q(int voff, srd_t srd, int soff, int lds) {
  unsigned keep;
  asm volatile("s_mov_b32 %0, m0\n\ts_mov_b32 exec_hi, 0x00ffffff\n\ts_mov_b32 m0, %4\n\ts_nop 0\n\t"
               "buffer_load_dwordx4 %1, %2, %3 offen lds\n\t"
               "s_mov_b32 m0, %0\n\ts_mov_b32 exec_hi, -1" : "=&s"(keep) : "v"(voff), "s"(srd), "s"(soff), "s"(lds) : "memory");
}
DI void lds_arrive(char* sm, int off, int l) { if (l == 0) __hip_atomic_fetch_add((unsigned*)(sm + off), 1u, __ATOMIC_RELAXED, __HIP_MEMORY_SCOPE_WORKGROUP); }
DI void lds_arrive_nb(int addr_lane0, int addr_other, int l) { asm volatile("ds_add_u32 %0, %1" :: "v"(l == 0 ? addr_lane0 : addr_other + l * 4), "v"(1) : "memory"); }
DI void lds_wait(int addr, unsigned n) {
  unsigned t;
  asm volatile("1:\n\tds_read_b32 %0, %1\n\ts_waitcnt lgkmcnt(0)\n\tv_cmp_gt_u32 vcc, %2, %0\n\ts_cbranch_vccnz 1b" : "=&v"(t) : "v"(addr), "s"(n) : "vcc", "memory");
}
DI void dma_full(int voff, srd_t srd, int soff, int lds) {
  unsigned keep;
  asm volatile("s_mov_b32 %0, m0\n\ts_mov_b32 m0, %4\n\ts_nop 0\n\tbuffer_load_dwordx4 %1, %2, %3 offen lds\n\ts_mov_b32 m0, %0" : "=&s"(keep) : "v"(voff), "s"(srd), "s"(soff), "s"(lds) : "memory");
}
DI v2u tr_half(const char* sm, int off) { return __builtin_bit_cast(v2u, __builtin_amdgcn_ds_read_tr16_b64_v4i16((LAS s4v*)((LAS char*)sm + off))); }
DI void red_write(char* sm, int off, const f16v& a0, const f16v& a1) {
#pragma unroll
  for (int q = 0; q < 4; q++) {
    lds_wr(sm, off + q * 1024, f4u(a0[4 * q], a0[4 * q + 1], a0[4 * q + 2], a0[4 * q + 3]));
    lds_wr(sm, off + (4 + q) * 1024, f4u(a1[4 * q], a1[4 * q + 1], a1[4 * q + 2], a1[4 * q + 3]));
  }
}
DI v4u red_final(const char* sm, int off, v4u res) {
  v4u t[16];
#pragma unroll
  for (int i = 0; i < 16; i++) t[i] = lds_rd(sm, off + i * 4096);
  SB();
  f4 e = {bflo(res[0]), bflo(res[1]), bflo(res[2]), bflo(res[3])}, o = {bfhi(res[0]), bfhi(res[1]), bfhi(res[2]), bfhi(res[3])};
#pragma unroll
  for (int w = 0; w < 8; w++) { e += __builtin_bit_cast(f4, t[2 * w]); o += __builtin_bit_cast(f4, t[2 * w + 1]); }
  v4u out;
#pragma unroll
  for (int i = 0; i < 4; i++) out[i] = pk2(e[i], o[i]);
  return out;
}

constexpr int L_X = 0;
constexpr int L_KV = 57344;
constexpr int L_KVIN = 114688;
constexpr int L_WO = 57344;
constexpr int L_OB0 = 0, L_OB1 = 114688;
constexpr int L_H0 = 0;
constexpr int L_H1 = 65536;
constexpr int L_HST = 131328;
constexpr int L_XN = 149504;
constexpr int L_MISC = 157696;
constexpr int L_DUMMY = 157824;
constexpr int L_BYTES = 158080;

#define ABL 0
#define DLY_X 20
#define DLY_AV 10
#define AV2AT 14
#define WARM 0
#define ROTD 0
#define TFIRST 0
#define UPOS 0
#define UNT " nt"
#define DLY_KQ 4
#define J2AT 9
#define FAAT 23
#define ROTC 0
#define DLY_PQ 0
#define DLY_KF 0
#define DLY_F1 8
#define DLY_KV 24
#define NPF0 6
#define NPF1 12
#define NPF2 9
#define NPF3 15
#define NWD0 8

__global__ __launch_bounds__(512, 2) void fused_block(Ptrs p) {
  __shared__ __attribute__((aligned(16))) char sm[L_BYTES];
  STAMP(0);
  const rsrc_t ws = mk(p.ws, WS_BYTES);
  const int b = blockIdx.x, xcd = b & 7, idx = b >> 3;
  const int w = __builtin_amdgcn_readfirstlane(threadIdx.x >> 6), l = threadIdx.x & 63, l16 = l * 16;
  unsigned ep, ut[8];
  {
    unsigned long long pc; int len;
    asm volatile("s_getpc_b64 %0\n.Lfb_pc:\n\ts_mov_b32 %1, .Lfb_end-.Lfb_pc" : "=s"(pc), "=s"(len));
    const void* eb = w == 0 ? (const void*)p.ws : (w == 1 ? p.x : (w == 2 ? p.wqkv : (w == 3 ? p.wo : (const void*)pc)));
    const int en = w == 0 ? 64 : (w == 4 ? len : (!TFIRST || w > 4 ? 0 : (w == 1 ? S * D * 2 : (w == 2 ? D * 3 * D * 2 : D * D * 2))));
    const int nl = w == 1 ? 56 : (w == 2 ? 37 : (w == 3 ? 13 : 32)), e0 = (w == 4 ? (idx & 7) : b) * nl;
    asm volatile("buffer_load_dword %0, %1, %2, 0 offen sc1" : "=v"(ep) : "v"(w == 0 ? 0 : (l < nl ? (e0 + l) * 128 : OOB)), "s"(mks(eb, en)) : "memory");
  }
  const int uks = 7 * ((b >> 1) & 3) + (w ? w - 1 : 0);
  asm volatile("s_barrier" ::: "memory");
  {
    const srd_t us = mks((b & 1) ? p.wu : p.wg, D * F * 2);
    const int uv = (b < 224 && w) ? (l >> 5) * 28672 + (l & 31) * 4 : OOB, uo = uks * 57344 + idx * 128;
#pragma unroll
    for (int j = 0; j < 8; j++) asm volatile("buffer_load_dword %0, %1, %2, %3 offen" UNT : "=v"(ut[j]) : "v"(uv), "s"(us), "s"(uo + j * 3584) : "memory");
  }
  const int t64 = 8 * (xcd & 3) + (idx & 7), hp = xcd < 4 ? 3 - (idx >> 3) : 4 + (idx >> 3);
  const int rot = wrap((idx & 7) + 2 * (idx >> 3), 7) % 7;
  v4u xg[7];
  {
    const srd_t xs = mks(p.x, S * D * 2);
    const int xv = (l >> 3) * 896 + (l & 7) * 16, xso = (t64 * 64 + w * 8) * 896;
#pragma unroll
    for (int i = 0; i < 7; i++) asm volatile("buffer_load_dwordx4 %0, %1, %2, %3 offen" : "=v"(xg[i]) : "v"(xv), "s"(xs), "s"(xso + wrap(i + rot, 7) * 128) : "memory");
  }
  asm volatile("s_barrier" ::: "memory");
  const srd_t wsrd = mks(p.wqkv, D * 3 * D * 2);
  const int l7 = (l * 37) >> 8, wv = l7 * 2688 + (l - 7 * l7) * 16 + hp * 112;
  const int lbase = (int)(size_t)(LAS char*)sm;
#define DMA_KV(c) dma_kv(wv, wsrd, (wrap((c) + rot, 7) * 8 + w) * 21504, lbase + L_KV + ((c) * 8 + w) * 1792)
#define DMA_Q(c) dma_q(wv, wsrd, (wrap((c) + rot, 7) * 8 + w) * 21504, lbase + L_X + ((c) * 8 + w) * 896)
  const int th2 = idx >= 14, tB = 2 * xcd + th2, m = idx - 14 * th2, T = m >> 1, a = m & 1;
  const bool ffn = idx < 28;
  const int r = l & 31, c16 = l & 15, g4 = l >> 4, hh = l >> 5;
  const v4u sent = {SENT, SENT, SENT, SENT};
  const int tt = w & 1, hd = (w >> 1) & 1;
  const int pq_off = OFF_PQ + ((2 * t64 + tt) * 16 + 2 * hp + hd) * 2048 + l16;
  const int part_off = OFF_PART + ((2 * hp + hd) * 32 + t64) * 4096 + (w & 1) * 2048 + l16;
  DMA_KV(0);
  SB();
  const int ai = idx, tC = 4 * (xcd & 3) + (ai & 3), cC = 7 * (xcd >> 2) + (ai >> 2);
  const int jcs = b - 224;
  *(v4u*)(sm + L_MISC + (threadIdx.x == 0 ? 0 : (threadIdx.x == 1 ? 32 : 16))) = v4u{0u, 0u, 0u, 0u};
  SB();
  asm volatile("s_waitcnt vmcnt(%8)" : "+v"(ep), "+v"(xg[0]), "+v"(xg[1]), "+v"(xg[2]), "+v"(xg[3]), "+v"(xg[4]), "+v"(xg[5]), "+v"(xg[6]) : "i"(UPOS == 1 ? 10 : 2));
  STAMP(1);
  DMA_KV(1);
  {
    float s = 0.f, ss = 0.f;
#pragma unroll
    for (int i = 0; i < 7; i++)
#pragma unroll
      for (int j = 0; j < 4; j++) { s = dot2(xg[i][j], ONES, s); ss = dot2(xg[i][j], xg[i][j], ss); }
    SB(); DMA_KV(2); SB();
    s = dpp_add<0x141>(dpp_add<0x4E>(dpp_add<0xB1>(s))); ss = dpp_add<0x141>(dpp_add<0x4E>(dpp_add<0xB1>(ss)));
    const float mean = s * (1.f / D), var = fmaxf(ss * (1.f / D) - mean * mean, 0.f);
    const float ra = __builtin_amdgcn_rsqf(var + 1e-5f), rb = -mean * ra;
    const int o = L_X + w * 7168 + (l >> 3) * 128 + (((l & 7) ^ (4 * (w & 1) + (l >> 4))) * 16);
#pragma unroll
    for (int i = 0; i < 7; i++) {
      float f[8];
#pragma unroll
      for (int j = 0; j < 4; j++) { f[2 * j] = bflo(xg[i][j]); f[2 * j + 1] = bfhi(xg[i][j]); }
#pragma unroll
      for (int j = 0; j < 8; j++) { f[j] = __builtin_fmaf(f[j], ra, rb); asm volatile("" : "+v"(f[j])); }
      const v4u xn = {pk2(f[0], f[1]), pk2(f[2], f[3]), pk2(f[4], f[5]), pk2(f[6], f[7])};
      lds_wr(sm, o + i * 1024, xn);
      if (i == 3) { SB(); DMA_KV(3); SB(); }
    }
  }
  *(unsigned*)(sm + L_MISC + (threadIdx.x == 0 ? 0 : 16)) = ep + 1u;
  SB();
  DMA_KV(4);
  BAR();
  STAMP(2);
  v4u xf[28];
  {
    const int row = 32 * tt + r, xrb = L_X + (row >> 3) * 7168 + (row & 7) * 128, sw = (row >> 1) & 7;
    int xa[4];
#pragma unroll
    for (int i = 0; i < 4; i++) xa[i] = xrb + (((2 * i + hh) ^ sw) * 16);
#pragma unroll
    for (int i = 0; i < 28; i++) xf[i] = lds_rd(sm, xa[i & 3] + (i >> 2) * 1024);
  }
  const unsigned tag = *(const unsigned*)(sm + L_MISC);
  const int trl = (c16 >> 2) * 112 + hd * 56 + min(4 * (g4 & 1) + (c16 & 3), 6) * 8;
  f16v acc = {};
  const int ba = fresh(L_KV + (w >> 2) * 896 + hh * 1792 + trl), bb2 = fresh(L_KV + (w >> 2) * 896 + hh * 1792 + trl + 57344);
#define RD_KV(i) tr_frag(sm, (i) < 16 ? ba + (i) * 3584 : bb2 + ((i) - 16) * 3584)
  v4u bb[4];
  {
    SB();
    {
      asm volatile("s_waitcnt vmcnt(8)" : "+v"(ut[0]), "+v"(ut[1]), "+v"(ut[2]), "+v"(ut[3]), "+v"(ut[4]), "+v"(ut[5]), "+v"(ut[6]), "+v"(ut[7]));
      const int d0 = w ? (b < 224 ? OFF_WC + (116 * idx + uks) * 1024 + (g4 & 1) * 59392 + hh * 512 + (b & 1) * 256 + c16 * 16 : OOB) : (ffn ? OOB : l16 + OFF_KVF + jcs * 1024);
      const int d1 = w ? d0 + 29696 : ((ffn && l < 32) ? l16 + OFF_HS + ((tB * 8 + T) * 2 + a) * 512 : OOB);
      const unsigned f_ = w ? 0u : SENT;
      stc(v4u{lo2(ut[0], ut[1]), lo2(ut[2], ut[3]), lo2(ut[4], ut[5]), lo2(ut[6], ut[7])} | f_, ws, d0);
      stc(v4u{hi2(ut[0], ut[1]), hi2(ut[2], ut[3]), hi2(ut[4], ut[5]), hi2(ut[6], ut[7])} | f_, ws, d1);
      SB(); DMA_KV(5); SB();
      stc(sent, ws, pq_off + (w >> 2) * 1024);
      stc(sent, ws, ffn ? l16 + OFF_HF + ((4 * tB + a + 2 * (w >> 2)) * 28 + 4 * T + (w & 3)) * 1024 : OOB);
      SB(); DMA_KV(6);
    }
    SB(); asm volatile("s_waitcnt vmcnt(10) lgkmcnt(0)\n\ts_barrier" ::: "memory"); SB();
#pragma unroll
    for (int i = 0; i < 3; i++) bb[i] = RD_KV(i);
    sfor<0, 13>([&](auto II) {
      constexpr int i = decltype(II)::v;
      if constexpr (i % 7 == 0) __builtin_amdgcn_s_setprio(3 - i / 7);
      if constexpr (i == 4 || i == 8 || i == 12) DMA_Q(i / 4 - 1);
      bb[(i + 3) & 3] = RD_KV(i + 3);
      acc = mfma32(xf[i], bb[i & 3], acc);
      SB();
    });
    asm volatile("s_waitcnt vmcnt(3) lgkmcnt(0)\n\ts_barrier" ::: "memory"); SB();
    STAMP(3);
  }
  v4u wc[29];
  const int wc_off = OFF_WC + (8 * cC + w) * 29 * 1024, rq = ROTC ? (ai & 3) : 0;
#define LD_WC(i) wc[i] = ldf(ws, lo, wc_off + ((((i) / 7) + rq) & 3) * 7168, (i) % 7)
  const srd_t os = mks(ffn ? p.wo : p.wd, ffn ? D * D * 2 : F * D * 2);
  const int ov = ffn ? (l >> 3) * 896 + (((l & 7) ^ (2 * ((l >> 4) & 3))) * 16) + 128 * T : (jcs < 28 ? l16 + jcs * 57344 : OOB), og = ffn ? 14 * (2 * th2 + a) : 0, ost = ffn ? 7168 : 1024;
#define DMA_WO(g) do { const int gg_ = wrap((g) + og, 56); dma_full(ov, os, gg_ * ost, lbase + L_WO + gg_ * 1024); } while (0)
  if (w < 4) {
    __builtin_amdgcn_s_setprio(3);
    sfor<13, 28>([&](auto II) {
      constexpr int i = decltype(II)::v;
      if constexpr (i == 21) { SB(); lds_arrive_nb(lbase + L_MISC + 32, lbase + L_DUMMY, l); SB(); }
      if constexpr (i + 3 < 28) bb[(i + 3) & 3] = RD_KV(i + 3);
      acc = mfma32(xf[i], bb[i & 3], acc);
      SB();
    });
    STAMP(4);
    {
      v4u fr[2];
#pragma unroll
      for (int j = 0; j < 8; j++) fr[j >> 2][j & 3] = pk2(phi(acc[2 * j]), phi(acc[2 * j + 1]));
      SB(); lds_wait(lbase + L_MISC + 32, 8u);
      lds_wr(sm, L_KVIN + w * 2048 + l16, fr[0]); lds_wr(sm, L_KVIN + w * 2048 + 1024 + l16, fr[1]);
    }
    SB(); asm volatile("s_waitcnt vmcnt(0) lgkmcnt(0)\n\ts_barrier" ::: "memory"); SB();
    STAMP(5);
    {
      v4u ka[2], vb[2][2];
      const int ko = L_KVIN + hd * 4096 + g4 * 512 + c16 * 16;
#pragma unroll
      for (int t_ = 0; t_ < 2; t_++) { ka[t_] = lds_rd(sm, ko + t_ * 2048 + (w & 1) * 256); vb[t_][0] = lds_rd(sm, ko + 8192 + t_ * 2048); vb[t_][1] = lds_rd(sm, ko + 8192 + t_ * 2048 + 256); }
      SB();
      f4 kv[2] = {};
#pragma unroll
      for (int t_ = 0; t_ < 2; t_++) { kv[0] = mfma16(ka[t_], vb[t_][0], kv[0]); kv[1] = mfma16(ka[t_], vb[t_][1], kv[1]); }
      stc(tag4(kv[0], tag), ws, part_off); stc(tag4(kv[1], tag), ws, part_off + 1024);
    }
    STAMP(6);
    if (w == 3) {
      wait_flags(ws, OFF_FS + xcd * 8192, ~0ull, l, tag, 0 DBGP);
      if (b == 0 && l == 0) __builtin_amdgcn_raw_buffer_store_b32(tag, ws, 0, OFF_EPOCH, SC1);
      lds_arrive(sm, L_MISC + 4, l);
    } else if (ffn) {
      __builtin_amdgcn_s_setprio(0);
#pragma nounroll
      for (int g = w; g < 56; g += 3) DMA_WO(g);
    }
    __builtin_amdgcn_s_setprio(0);
  } else {
    __builtin_amdgcn_s_setprio(0);
    sfor<13, 28>([&](auto II) {
      constexpr int i = decltype(II)::v;
      if constexpr (i == 13) set_flag(ws, OFF_FS, b, w == 7, l, tag);
      if constexpr (i == 21) { SB(); lds_arrive_nb(lbase + L_MISC + 32, lbase + L_DUMMY, l); SB(); }
      if constexpr (i == 15 || i == 18 || i == 22 || i == 25) {
        constexpr int c = i == 15 ? 3 : (i == 18 ? 4 : (i == 22 ? 5 : 6));
        dma_q(wv, wsrd, (wrap(c + rot, 7) * 8 + w - 4) * 21504, lbase + L_X + (c * 8 + w - 4) * 896); DMA_Q(c);
      }
      if constexpr (i + 3 < 28) bb[(i + 3) & 3] = RD_KV(i + 3);
      acc = mfma32(xf[i], bb[i & 3], acc);
      SB();
    });
    STAMP(4);
    {
      const unsigned pad = r == 28 ? ONES : 0u;
      v4u fr[2];
#pragma unroll
      for (int j = 0; j < 8; j++) { const unsigned t_ = pk2(acc[2 * j], acc[2 * j + 1]); fr[j >> 2][j & 3] = r < 28 ? t_ : pad; }
      SB(); lds_wait(lbase + L_MISC + 32, 8u);
      lds_wr(sm, L_KVIN + w * 2048 + l16, fr[0]); lds_wr(sm, L_KVIN + w * 2048 + 1024 + l16, fr[1]);
    }
    BAR();
    STAMP(5);
    asm volatile("s_waitcnt vmcnt(0)" ::: "memory");
    lds_arrive(sm, L_MISC + 36, l);
    lds_wait(lbase + L_MISC + 36, 4u);
    const int qa = fresh(L_X + hh * 896 + trl);
    f16v aq = {};
#pragma unroll
    for (int i = 0; i < 3; i++) bb[i] = tr_frag(sm, qa + i * 1792);
    SB();
    sfor<0, 28>([&](auto II) {
      constexpr int i = decltype(II)::v;
      if constexpr (i + 3 < 28) bb[(i + 3) & 3] = tr_frag(sm, qa + (i + 3) * 1792);
      aq = mfma32(bb[i & 3], xf[i], aq);
      SB();
    });
    __builtin_amdgcn_s_setprio(3);
#pragma unroll
    for (int s = 0; s < 2; s++) {
      v4u pf = {pk2(phi(aq[8 * s]), phi(aq[8 * s + 1])), pk2(phi(aq[8 * s + 2]), phi(aq[8 * s + 3])), pk2(phi(aq[8 * s + 4]), phi(aq[8 * s + 5])), pk2(phi(aq[8 * s + 6]), phi(aq[8 * s + 7]))};
      if (s == 1) { pf[2] = hh ? 0u : pf[2]; pf[3] = hh ? 0u : pf[3]; }
      stc(canon(pf), ws, pq_off + s * 1024);
    }
    __builtin_amdgcn_s_setprio(0);
    STAMP(8);
  }
  SB();
  STAMP(10);
  if (!ffn) {
    {
      const int rh = jcs >> 1, rj = (jcs & 1) * 8 + w;
      v4u pv[8];
      const int so = OFF_PART + rh * 131072 + ((rj >> 3) * 2 + ((rj >> 1) & 1)) * 1024 + ((rj >> 2) & 1) * 256 + (rj & 1) * 128;
      const unsigned tb = tag & 0xffu;
      if (w < 4) __builtin_amdgcn_s_sleep(DLY_KV); else __builtin_amdgcn_s_sleep(DLY_KQ);
#pragma nounroll
      for (int spin = 0; spin < SPIN_CAP; spin++) {
        MEMBAR();
        const int vo = fresh(g4 * 4096 + (c16 >> 3) * 512 + (c16 & 7) * 16);
        unsigned mm = 0;
#pragma unroll
        for (int i = 0; i < 8; i++) pv[i] = ldf(ws, vo, so + i * 16384, 0);
#pragma unroll
        for (int i = 0; i < 8; i++) { pv[i] ^= tb; mm |= (pv[i][0] | pv[i][1]) | (pv[i][2] | pv[i][3]); }
        if (__builtin_amdgcn_ballot_w64((mm & 0xffu) != 0u) == 0) break;
        DBG(3);
      }
      f4 s = __builtin_bit_cast(f4, pv[0]);
#pragma unroll
      for (int i = 1; i < 8; i++) s += __builtin_bit_cast(f4, pv[i]);
#pragma unroll
      for (int i = 0; i < 4; i++) s[i] = sum32(sum16(s[i]));
      const v2u o = {min(pk2(s[0], s[1]), SENT - 1u), min(pk2(s[2], s[3]), SENT - 1u)};
      __builtin_amdgcn_raw_buffer_store_b64(o, ws, l < 16 ? (c16 & 7) * 16 + (c16 >> 3) * 8 : OOB, OFF_KVF + jcs * 1024 + w * 128, SC1);
      STAMP(9);
      BAR();
    }
    if (jcs < 28) {
#pragma unroll
      for (int i = 0; i < 7; i++) DMA_WO(8 * i + w);
      const int ft = 4 * jcs + (w >> 1), lo = fresh(l16);
      v4u v[14]; float cs = 0.f;
#pragma unroll
      for (int i = 0; i < 14; i++) v[i] = ldf(ws, lo, OFF_WC + (ft * 29 + 14 * (w & 1)) * 1024, i);
#pragma unroll
      for (int i = 0; i < 14; i++)
#pragma unroll
        for (int j = 0; j < 4; j++) cs = dot2(v[i][j], ONES, cs);
      cs = sum32(cs);
      *(float*)(sm + L_HST + w * 256 + l * 4) = cs;
      BAR();
      if (w < 7) {
        unsigned u_[4][8];
        const int dv = fresh(L_WO + hh * 7168 + r * 4 + 128 * w);
#pragma unroll
        for (int u = 0; u < 4; u++)
#pragma unroll
          for (int j = 0; j < 8; j++) u_[u][j] = *(const unsigned*)(sm + dv + (u & 1) * 896 + (u >> 1) * 28672 + (j & 3) * 1792 + (j >> 2) * 14336);
        SB();
#pragma unroll
        for (int u = 0; u < 4; u++) {
          const int d0 = OFF_WD + (224 * w + 4 * jcs + u) * 1024 + l16;
          stc(v4u{lo2(u_[u][0], u_[u][1]), lo2(u_[u][2], u_[u][3]), lo2(u_[u][4], u_[u][5]), lo2(u_[u][6], u_[u][7])}, ws, d0);
          stc(v4u{hi2(u_[u][0], u_[u][1]), hi2(u_[u][2], u_[u][3]), hi2(u_[u][4], u_[u][5]), hi2(u_[u][6], u_[u][7])}, ws, d0 + 114688);
        }
      }
      if (!(w & 1)) {
        cs += *(const float*)(sm + L_HST + (w + 1) * 256 + l * 4);
        const unsigned hi = pk2(cs, cs), lw = pk2(cs - bflo(hi), 0.f);
        const v4u fr = {l < 32 ? hi : 0u, l < 32 ? lw : 0u, 0u, 0u};
        stc(fr, ws, l16 + OFF_WC + (ft * 29 + 28) * 1024);
      }
      asm volatile("s_waitcnt vmcnt(0)" ::: "memory");
      BAR();
    }
    if (w == 0) set_flag(ws, OFF_FD, b, true, l, tag);
  } else {
    const int c4 = w & 3, th = w >> 2;
    v4u wo[16], pq[8], kvf[4], hv[8];
    int woa[2];
#pragma unroll
    for (int e = 0; e < 2; e++) woa[e] = L_WO + (16 * (g4 >> 1) + 4 * (g4 & 1) + (c16 >> 2)) * 128 + (((2 * c4 + ((c16 >> 1) & 1)) ^ (2 * ((((e + g4) & 1) << 1) + (c16 >> 3)))) * 16) + (c16 & 1) * 8;
    const int wo8 = g4 == 3 ? 0 : 1024;
    const int hoff = OFF_HF + (4 * tC * 28 + 4 * w) * 1024;
    v2u xr[2];
    lds_wait(lbase + L_MISC + 4, 1u);
    {
      const int lo = fresh(l16);
#pragma unroll
      for (int ps = 0; ps < 2; ps++) xr[ps] = *(const v2u*)((const char*)p.x + (size_t)((4 * tB + a + 2 * ps) * 32 + 16 * th + c16) * 896 + (64 * T + 16 * c4 + 4 * g4) * 2);
#pragma unroll
      for (int i = 0; i < NPF0; i++) LD_WC(i);
      SB();
      asm volatile("s_waitcnt vmcnt(%0)" :: "i"(NPF0 + 2) : "memory");
      lds_arrive(sm, L_MISC + 8, l);
    }
    lds_wait(lbase + L_MISC + 8, 8u);
    __builtin_amdgcn_s_setprio(2);
    poll8(pq, ws, l16, OFF_PQ + ((4 * tB + a) * 32 + 4 * w) * 1024, OFF_PQ + ((4 * tB + a + 2) * 32 + 4 * w) * 1024, 4 DBGP, [&] {
      const int lo = fresh(l16);
      __builtin_amdgcn_s_setprio(0);
#pragma unroll
      for (int i = 0; i < 16; i++) {
        const v2u x0 = tr_half(sm, woa[i & 1] + i * 3584), x1 = tr_half(sm, woa[i & 1] + wo8 + i * 3584);
        wo[i] = v4u{x0[0], x0[1], x1[0], x1[1]};
      }
      SB();
      if (DLY_KF) __builtin_amdgcn_s_sleep(DLY_KF);
      __builtin_amdgcn_s_setprio(3);
#pragma unroll
      for (int i = 0; i < 4; i++) kvf[i] = ldf(ws, lo, OFF_KVF + 4 * w * 1024, i);
      __builtin_amdgcn_s_setprio(0);
      SB();
#pragma unroll
      for (int i = NPF0; i < NPF1; i++) LD_WC(i);
    });
    STAMP(7);
    {
      unsigned mm = 0;
#pragma unroll
      for (int i = 0; i < 4; i++) mm = mx4(mm, kvf[i]);
      if (bad(mm)) { DBG(5); __builtin_amdgcn_s_setprio(3); poll_frags<4>(kvf, ws, l16, OFF_KVF + 4 * w * 1024, 5 DBGP); __builtin_amdgcn_s_setprio(0); }
    }
    STAMP(11);
    sfor<0, 2>([&](auto PS) {
      constexpr int ps = decltype(PS)::v;
      const int lo = fresh(l16);
      f16v n[2] = {};
#pragma unroll
      for (int s = 0; s < 2; s++)
#pragma unroll
        for (int hd = 0; hd < 2; hd++) n[hd] = mfma32(kvf[2 * hd + s], pq[4 * ps + 2 * hd + s], n[hd]);
      constexpr int pa = ps ? NPF1 + 4 : NPF1;
      LD_WC(pa); LD_WC(pa + 1);
      SB();
#pragma unroll
      for (int hd = 0; hd < 2; hd++) {
        const auto sw = __builtin_amdgcn_permlane32_swap(__float_as_uint(n[hd][12]), __float_as_uint(n[hd][12]), false, false);
        const float inv = __builtin_amdgcn_rcpf(__uint_as_float(sw[1]) + 1e-6f);
        v4u o0, o1;
#pragma unroll
        for (int i = 0; i < 4; i++) { o0[i] = pk2(n[hd][2 * i] * inv, n[hd][2 * i + 1] * inv); o1[i] = pk2(n[hd][8 + 2 * i] * inv, n[hd][9 + 2 * i] * inv); }
        o1[2] = hh ? 0u : o1[2];
        lds_wr(sm, (ps ? L_OB1 : L_OB0) + (4 * w + 2 * hd) * 1024 + lo, o0);
        lds_wr(sm, (ps ? L_OB1 : L_OB0) + (4 * w + 2 * hd + 1) * 1024 + lo, o1);
      }
      BAR();
      if (ps == 0) STAMP(12);
      v4u ob[8];
      const int oo = fresh((ps ? L_OB1 : L_OB0) + g4 * 512 + th * 256 + c16 * 16), vh = fresh(w < 7 ? l16 : OOB);
#pragma unroll
      for (int i = 0; i < 8; i++) ob[i] = lds_rd(sm, oo + i * 2048);
      f4 hacc = {bflo(xr[ps][0]), bfhi(xr[ps][0]), bflo(xr[ps][1]), bfhi(xr[ps][1])};
      SB();
#pragma unroll
      for (int i = 0; i < 16; i++) {
        hacc = mfma16(wo[i], ob[i & 7], hacc);
        if (i < 8) ob[i] = lds_rd(sm, oo + (8 + i) * 2048);
        if (ps == 1 && (i & 1)) hv[i >> 1] = ldf(ws, vh, hoff + (i >> 3) * 28 * 1024, (i >> 1) & 3);
        SB();
      }
      const unsigned p0 = min(pk2(hacc[0], hacc[1]), SENT - 1u), p1 = min(pk2(hacc[2], hacc[3]), SENT - 1u);
      const auto s0 = __builtin_amdgcn_permlane16_swap(p0, p0, false, false), s1 = __builtin_amdgcn_permlane16_swap(p1, p1, false, false);
      const v4u hf = {s0[0], s1[0], s0[1], s1[1]};
      stc(hf, ws, (g4 & 1) ? OOB : OFF_HF + ((4 * tB + a + 2 * ps) * 28 + 4 * T + c4) * 1024 + (g4 >> 1) * 512 + th * 256 + c16 * 16);
      STAMP(13 + ps);
      LD_WC(pa + 2); LD_WC(pa + 3);
      {
        const float s_ = dot2(p0, ONES, dot2(p1, ONES, 0.f)), ss_ = dot2(p0, p0, dot2(p1, p1, 0.f));
        *(f2*)(sm + L_XN + ((c4 * 4 + g4) * 32 + 16 * th + c16) * 16 + ps * 8) = f2{s_, ss_};
      }
      SB();
    });
    v4u wd[28], av[14];
    const int rd = ROTD ? 2 * wrap(wrap(T + 2 * (2 * th2 + a), 7), 7) : 0;
    const int wd_off = OFF_WD + ((2 * T) * 112 + 14 * w) * 1024;
#define LD_WD(i) wd[i] = ldf(ws, (ABL & 1) ? OOB : lo, wd_off + (((i) & 1) * 112 + wrap((((i) >> 2) << 1) + rd, 14)) * 1024, ((i) >> 1) & 1)
#define LD_AV(i, off) av[i] = ldf(ws, (ABL & 4) ? OOB : lo, (off) + wrap(((i) & ~1) + rd, 14) * 1024, (i) & 1)
    const int g2a = w < 7 ? hoff + 56 * 1024 : OFF_HS + tC * 8192, g2b = w < 7 ? hoff + 84 * 1024 : OFF_HS + tC * 8192 + 3 * 1024;
    int hw[4];
#pragma unroll
    for (int i = 0; i < 4; i++) hw[i] = w < 7 ? wrap(4 * w + i + 28 - 7 * rq, 28) * 1024 : L_HST - L_H1 + i * 1024;
    const int hk = w < 7 ? 28 * 1024 : 3 * 1024;
    float c1[4], vv[4]; unsigned mneg[4];
    f4 sq[7];
    auto stats_read = [&] {
#pragma unroll
      for (int i = 0; i < 7; i++) sq[i] = *(const f4*)(sm + L_HST + i * 1024 + l16);
    };
    auto stats_math = [&](int ps) {
      float s_ = sq[0][2 * ps], ss_ = sq[0][2 * ps + 1];
#pragma unroll
      for (int i = 1; i < 7; i++) { s_ += sq[i][2 * ps]; PIN(s_); ss_ += sq[i][2 * ps + 1]; }
      const float nm = s_ * (-1.f / D), ve = fmaxf(ss_ * (1.f / D) - nm * nm, 0.f) + 1e-5f;
      const float cc = __builtin_amdgcn_rsqf(ve) * -1.44269504089f;
      const unsigned mh = pk2(nm, 0.f), mm = pk2(nm, nm - bflo(mh));
      const auto s1 = __builtin_amdgcn_permlane32_swap(__float_as_uint(cc), __float_as_uint(cc), false, false);
      const auto s2 = __builtin_amdgcn_permlane32_swap(__float_as_uint(ve), __float_as_uint(ve), false, false);
      const auto s3 = __builtin_amdgcn_permlane32_swap(mm, mm, false, false);
#pragma unroll
      for (int k = 0; k < 2; k++) { c1[2 * ps + k] = __uint_as_float(s1[k]); vv[2 * ps + k] = __uint_as_float(s2[k]); mneg[2 * ps + k] = s3[k]; }
    };
    BAR();
    asm volatile("" : "+v"(hv[0]), "+v"(hv[1]), "+v"(hv[2]), "+v"(hv[3]), "+v"(hv[4]), "+v"(hv[5]), "+v"(hv[6]), "+v"(hv[7]));
    if (w < 7) {
      unsigned mm = 0;
#pragma unroll
      for (int i = 0; i < 8; i++) mm = mx4(mm, hv[i]);
      if (bad(mm)) { DBG(6); poll8(hv, ws, l16, hoff, hoff + 28 * 1024, 6 DBGP, [] {}); }
      STAMP(15);
#pragma unroll
      for (int i = 0; i < 8; i++) lds_wr(sm, L_H0 + l16 + hw[i & 3] + (i >> 2) * 28 * 1024, hv[i]);
    } else {
      f4 t = {};
#pragma unroll
      for (int i = 0; i < 8; i++) t += *(const f4*)(sm + L_XN + i * 1024 + l16);
#pragma unroll
      for (int i = 0; i < 4; i++) t[i] = sum32(t[i]);
      stc(canon(__builtin_bit_cast(v4u, t)), ws, l < 32 ? l16 + OFF_HS + ((tB * 8 + T) * 2 + a) * 512 : OOB);
    }
    BAR();
    STAMP(16);
    const int ftt = 4 * tB + a + 2 * (w >> 2);
    v4u res;
    const int a0off = OFF_ACT + ((4 * tB + a) * 112 + 14 * w) * 1024;
    const srd_t srd = mks(p.ws, WS_BYTES);
    const int fdo = (56 + (r >> 2)) * 128 + (l & 3) * 4;
    f16v ac[2][2] = {};
    float eo[2]; v4u af;
    auto epi = [&](auto PS, auto EE) {
      constexpr int ps = decltype(PS)::v, e = decltype(EE)::v, k = e >> 3, j = e & 7;
      float g = ac[ps][k][j], t = g * c1[2 * ps + k]; PIN(t);
      float gu = g * ac[ps][k][8 + j]; PIN(gu);
      eo[j & 1] = gu * __builtin_amdgcn_rcpf(__builtin_fmaf(__builtin_amdgcn_exp2f(t), vv[2 * ps + k], vv[2 * ps + k]));
      PIN(eo[j & 1]);
      if constexpr (j & 1) af[j >> 1] = min(pk2(eo[0], eo[1]), SENT - 1u);
      if constexpr (j == 7) stc(af, ws, l16 + OFF_ACT + ((4 * tC + 2 * ps + k) * 112 + 8 * cC + w) * 1024);
    };
    auto fold_mean = [&](int ps) {
#pragma unroll
      for (int k = 0; k < 2; k++) { const v4u bx = {mneg[2 * ps + k], mneg[2 * ps + k] & 0xffffu, 0u, 0u}; ac[ps][k] = mfma32(wc[28], bx, ac[ps][k]); }
    };
    sfor<0, 2>([&](auto PS) {
      constexpr int ps = decltype(PS)::v;
      const int lo = fresh(l16);
      const int hb = fresh((ps ? L_H1 : L_H0) + l16);
      v4u bb[3][2];
      unsigned fa = 0, mm = 0;
#pragma unroll
      for (int i = 0; i < 2; i++) { bb[i][0] = lds_rd(sm, hb + i * 1024); bb[i][1] = lds_rd(sm, hb + (28 + i) * 1024); }
      if constexpr (ps == 1) { fold_mean(0); fold_mean(1); }
      SB();
      sfor<0, 28>([&](auto II) {
        constexpr int i = decltype(II)::v;
        if constexpr (i % 7 == 0) __builtin_amdgcn_s_setprio(3 - i / 7);
        if constexpr (i % 4 == 0) STAMPX(1 + ps, 1 + i / 4);
        sfor<0, 2>([&](auto KK) {
          constexpr int k = decltype(KK)::v;
          if constexpr (i + 2 < 28) bb[(i + 2) % 3][k] = lds_rd(sm, hb + (k * 28 + i + 2) * 1024);
          ac[ps][k] = mfma32(wc[i], bb[i % 3][k], ac[ps][k]);
          if constexpr (ps == 0 && k == 0 && i >= 15 && i < 23) {
            constexpr int f = i - 15;
            mm = mx4(mm, hv[f]);
            lds_wr(sm, L_H1 + lo + hw[f & 3] + (f >> 2) * hk, hv[f]);
          }
          if constexpr (ps == 1 && i >= 1 && i < 5) { epi(IC<0>{}, IC<4 * (i - 1) + 2 * k>{}); epi(IC<0>{}, IC<4 * (i - 1) + 2 * k + 1>{}); }
          SB();
        });
        if constexpr (ps == 0) {
          if constexpr (NPF1 + 8 + 2 * i < 28) { LD_WC(NPF1 + 8 + 2 * i); LD_WC(NPF1 + 9 + 2 * i); }
          if constexpr (i >= 6 && i < 14) hv[i - 6] = ldf(ws, lo, i < 10 ? g2a : g2b, (i - 6) & 3);
          if constexpr (i == 14) fa = __builtin_amdgcn_raw_buffer_load_b32(ws, fdo, OFF_FD + xcd * 8192, SC1);
          if constexpr (i == 27 - NWD0) { if (__builtin_amdgcn_ballot_w64(fa != tag) != 0) DBG(2); wait_words(fa, tag, srd, fdo, OFF_FD + xcd * 8192); wc[28] = ldf(ws, lo, wc_off + 28 * 1024, 0); }
          if constexpr (i == 15 || i == 17) stc(sent, ws, lo + OFF_ACT + ((4 * tC + i / 2 - 5) * 112 + 8 * cC + w) * 1024);
          if constexpr (i >= 28 - NWD0) LD_WD(i - (28 - NWD0));
          if constexpr (i == 22) *(unsigned*)(sm + L_MISC + (mm == SENT ? 12 : 16)) = 1u;
          if constexpr (i == 23) { SB(); asm volatile("s_waitcnt lgkmcnt(2)\n\ts_barrier" ::: "memory"); SB(); stats_read(); fa = *(const unsigned*)(sm + L_MISC + 12); }
          if constexpr (i == 25) stats_math(0);
          if constexpr (i == 26) stats_math(1);
          SB();
        } else {
          if constexpr (NWD0 + i < 28) LD_WD(NWD0 + i);
          if constexpr (i == J2AT) {
            SB(); asm volatile("s_waitcnt vmcnt(%0)\n\ts_barrier" :: "i"(J2AT - 3) : "memory"); SB();
            __builtin_amdgcn_raw_buffer_store_b32(tag, ws, threadIdx.x == 0 ? 0 : OOB, OFF_FA + (tC * 16 + cC) * 128, SC1);
          }
          if constexpr (i == 20) res = ldf(ws, lo, OFF_HF + (ftt * 28 + 4 * T + (w & 3)) * 1024, 0);
          if constexpr (i == FAAT - 5) fa = __builtin_amdgcn_raw_buffer_load_b32(ws, l < 14 ? l * 128 : 0, OFF_FA + tB * 2048, SC1);
          if constexpr (i == FAAT) {
            if (__builtin_amdgcn_ballot_w64(fa != tag) != 0) DBG(10);
            wait_words(fa, tag, srd, l < 14 ? l * 128 : 0, OFF_FA + tB * 2048);
          }
          if constexpr (i >= FAAT) LD_AV(i - FAAT, a0off);
          SB();
        }
      });
      if constexpr (ps == 0) {
        if (__builtin_amdgcn_readfirstlane(fa) != 0u) {
          DBG(7);
          BAR();
          poll8(hv, ws, l16, g2a, g2b, 8 DBGP, [] {});
#pragma unroll
          for (int f = 0; f < 8; f++) lds_wr(sm, L_H1 + l16 + hw[f & 3] + (f >> 2) * hk, hv[f]);
          BAR();
          stats_read(); stats_math(0); stats_math(1);
        }
      }
      STAMP(17 + 2 * ps);
    });
    {
      int lo = fresh(l16);
      v4u av2[14];
      const rsrc_t os2 = mk(p.out, S * D * 2);
      const int oo = (ftt * 32 + r) * 896 + (64 * T + 16 * (w & 3) + 8 * (l >> 5)) * 2;
#define LD_AV2(i) av2[i] = ldf(ws, lo, a0off + 2 * 112 * 1024 + wrap(((i) & ~1) + rd, 14) * 1024, (i) & 1)
      {
        f16v acc0 = {}, acc1 = {};
        sfor<0, 14>([&](auto II) {
          constexpr int i = decltype(II)::v;
          if constexpr (i % 4 == 0) __builtin_amdgcn_s_setprio(3 - i / 4);
          if constexpr (28 - FAAT + 2 * i < 14) LD_AV(28 - FAAT + 2 * i, a0off);
          if constexpr (28 - FAAT + 2 * i + 1 < 14) LD_AV(28 - FAAT + 2 * i + 1, a0off);
          acc0 = mfma32(wd[2 * i], av[i], acc0);
          if constexpr (i < 4) { epi(IC<1>{}, IC<4 * i>{}); epi(IC<1>{}, IC<4 * i + 1>{}); }
          SB();
          acc1 = mfma32(wd[2 * i + 1], av[i], acc1);
          if constexpr (i < 4) { epi(IC<1>{}, IC<4 * i + 2>{}); epi(IC<1>{}, IC<4 * i + 3>{}); }
          SB();
          if constexpr (i == 3) { STAMP(20); asm volatile("s_barrier" ::: "memory"); SB(); }
          if constexpr (i >= AV2AT) { LD_AV2(i - AV2AT); SB(); }
        });
        STAMP(21);
        sfor<0, 8>([&](auto QQ) {
          constexpr int q = decltype(QQ)::v;
          if constexpr (14 - AV2AT + q < 14) { LD_AV2(14 - AV2AT + q); }
          const f16v& a_ = q < 4 ? acc0 : acc1;
          lds_wr(sm, L_H0 + w * 8192 + l16 + q * 1024, f4u(a_[4 * (q & 3)], a_[4 * (q & 3) + 1], a_[4 * (q & 3) + 2], a_[4 * (q & 3) + 3]));
          SB();
        });
#pragma unroll
        for (int i = 22 - AV2AT; i < 14; i++) LD_AV2(i);
        SB();
      }
      {
        f16v acc0 = {}, acc1 = {};
        sfor<0, 14>([&](auto II) {
          constexpr int i = decltype(II)::v;
          if constexpr (i % 4 == 0) __builtin_amdgcn_s_setprio(3 - i / 4);
          acc0 = mfma32(wd[2 * i], av2[i], acc0); acc1 = mfma32(wd[2 * i + 1], av2[i], acc1);
          SB();
        });
        if (__builtin_amdgcn_ballot_w64(acc0[0] != acc0[0]) != 0) {
          DBG(11);
#pragma nounroll
          for (int spin = 0; spin < SPIN_CAP; spin++) {
            MEMBAR();
            unsigned mm = 0;
            lo = fresh(l16);
#pragma unroll
            for (int i = 0; i < 14; i++) LD_AV2(i);
#pragma unroll
            for (int i = 0; i < 14; i++) mm = mx4(mm, av2[i]);
            if (!bad(mm)) break;
          }
          acc0 = f16v{}; acc1 = f16v{};
#pragma unroll
          for (int i = 0; i < 14; i++) { acc0 = mfma32(wd[2 * i], av2[i], acc0); acc1 = mfma32(wd[2 * i + 1], av2[i], acc1); }
        }
        STAMP(22);
        red_write(sm, L_H1 + w * 8192 + l16, acc0, acc1);
        BAR();
        PIN(res);
        __builtin_amdgcn_raw_buffer_store_b128(red_final(sm, fresh((w < 4 ? L_H0 : L_H1) + (w & 3) * 1024 + l16), res), os2, oo, 0, 0);
      }
    }
  }
  asm volatile(".Lfb_end:");
  STAMP(23);
}

extern "C" void kernel_launch(void* const* d_in, const int* in_sizes, int n_in, void* d_out, int out_size, void* d_ws, size_t ws_size, hipStream_t stream) {
  Ptrs p;
  p.x = d_in[0]; p.wqkv = d_in[1]; p.wo = d_in[2]; p.wg = d_in[4]; p.wu = d_in[5]; p.wd = d_in[6]; p.out = (char*)d_out; p.ws = (char*)d_ws;
  fused_block<<<256, 512, 0, stream>>>(p);
}
```

```cpp
#include <hip/hip_runtime.h>
#include <stdint.h>

constexpr int S = 2048, D = 448, F = 1792;
typedef unsigned v4u __attribute__((ext_vector_type(4)));
typedef unsigned v2u __attribute__((ext_vector_type(2)));
typedef float f4 __attribute__((ext_vector_type(4)));
typedef float f2 __attribute__((ext_vector_type(2)));
typedef float f16v __attribute__((ext_vector_type(16)));
typedef __bf16 bf2 __attribute__((ext_vector_type(2)));
typedef __bf16 bf8 __attribute__((ext_vector_type(8)));
typedef int srd_t __attribute__((ext_vector_type(4)));
typedef __amdgpu_buffer_rsrc_t rsrc_t;
#define DI __device__ __forceinline__
#define XNT ""
#define WNT ""
#define SB() __builtin_amdgcn_sched_barrier(0)
#define MEMBAR() asm volatile("" ::: "memory")
#define BAR() do { SB(); asm volatile("s_waitcnt lgkmcnt(0)\n\ts_barrier" ::: "memory"); SB(); } while (0)

constexpr int OFF_EPOCH = 0, OFF_FA = 32768;
constexpr int OFF_FS = 65536, OFF_FC = OFF_FS + 65536, OFF_FD = OFF_FC + 65536;
constexpr int OFF_WC = 262144;
constexpr int OFF_WD = OFF_WC + 112 * 29 * 1024;
constexpr int OFF_PART = OFF_WD + 1568 * 1024;
constexpr int OFF_KVF = OFF_PART + 2048 * 1024;
constexpr int OFF_PQ = OFF_KVF + 32 * 1024;
constexpr int OFF_HF = OFF_PQ + 2048 * 1024;
constexpr int OFF_HS = OFF_HF + 1792 * 1024;
constexpr int OFF_ACT = OFF_HS + 128 * 1024;
constexpr int WS_BYTES = OFF_ACT + 7168 * 1024;
constexpr int SET_BYTES = 20 << 20;
static_assert(WS_BYTES <= SET_BYTES, "");
constexpr int OOB = 0x40000000;
constexpr int SC1 = 16;
constexpr unsigned SENT = 0xFFFFFFFFu;
constexpr int SPIN_CAP = 1 << 16;

#define STAMP(i)
#define STAMPX(set, i)
#define DBG(i)
#define DBGA
#define DBGP

DI rsrc_t mk(const void* p, int bytes) { return __builtin_amdgcn_make_buffer_rsrc((void*)p, 0, bytes, 0x00020000); }
DI v4u ldf(rsrc_t r, int voff, int base, int i) { return __builtin_amdgcn_raw_buffer_load_b128(r, voff + (i & 3) * 1024, base + (i >> 2) * 4096, SC1); }
DI void stc(v4u v, rsrc_t r, int off) { __builtin_amdgcn_raw_buffer_store_b128(v, r, off, 0, SC1); }
DI v4u canon(v4u v) { v4u o; for (int i = 0; i < 4; i++) o[i] = min(v[i], SENT - 1u); return o; }
DI f16v mfma32(v4u a, v4u b, f16v c) { return __builtin_amdgcn_mfma_f32_32x32x16_bf16(__builtin_bit_cast(bf8, a), __builtin_bit_cast(bf8, b), c, 0, 0, 0); }
DI f4 mfma16(v4u a, v4u b, f4 c) { return __builtin_amdgcn_mfma_f32_16x16x32_bf16(__builtin_bit_cast(bf8, a), __builtin_bit_cast(bf8, b), c, 0, 0, 0); }
DI unsigned pk2(float lo, float hi) { f2 v = {lo, hi}; return __builtin_bit_cast(unsigned, __builtin_convertvector(v, bf2)); }
DI float dot2(unsigned a, unsigned b, float c) { return __builtin_amdgcn_fdot2_f32_bf16(__builtin_bit_cast(bf2, a), __builtin_bit_cast(bf2, b), c, false); }
DI float bflo(unsigned u) { return __uint_as_float(u << 16); }
DI float bfhi(unsigned u) { return __uint_as_float(u & 0xffff0000u); }
DI float phi(float t) { return (fmaxf(t, 0.f) + 1e-6f) + __builtin_amdgcn_fmed3f(__builtin_amdgcn_exp2f(t * 1.44269504089f), 0.f, 1.f); }
constexpr unsigned ONES = 0x3f803f80u;
DI v4u lds_rd(const char* sm, int off) { return *(const v4u*)(sm + off); }
DI void lds_wr(char* sm, int off, v4u v) { *(v4u*)(sm + off) = v; }
DI v4u f4u(float a, float b, float c, float d) { v4u r = {__float_as_uint(a), __float_as_uint(b), __float_as_uint(c), __float_as_uint(d)}; return r; }
DI unsigned mx4(unsigned m, v4u v) { return max(max(max(m, v[0]), v[1]), max(v[2], v[3])); }
DI bool bad(unsigned m) { return __builtin_amdgcn_ballot_w64(m == SENT) != 0; }
DI float sum16(float v) { auto s = __builtin_amdgcn_permlane16_swap(__float_as_uint(v), __float_as_uint(v), false, false); return __uint_as_float(s[0]) + __uint_as_float(s[1]); }
DI float sum32(float v) { auto s = __builtin_amdgcn_permlane32_swap(__float_as_uint(v), __float_as_uint(v), false, false); return __uint_as_float(s[0]) + __uint_as_float(s[1]); }
template <int CTRL> DI float dpp_add(float v) { return v + __builtin_bit_cast(float, __builtin_amdgcn_update_dpp(0, __builtin_bit_cast(int, v), CTRL, 0xf, 0xf, true)); }
DI float wave_sum(float v) { v = dpp_add<0xB1>(v); v = dpp_add<0x4E>(v); v = dpp_add<0x141>(v); v = dpp_add<0x140>(v); return sum32(sum16(v)); }
DI int fresh(int v) { asm volatile("" : "+v"(v)); return v; }
#define PIN(x) asm volatile("" : "+v"(x))
DI int sfresh(int v) { asm volatile("" : "+s"(v)); return v; }
DI int wrap(int v, int n) { return v >= n ? v - n : v; }
template <int I> struct IC { static constexpr int v = I; };
template <int I, int N, class Fn> DI void sfor(Fn&& f) { if constexpr (I < N) { f(IC<I>{}); sfor<I + 1, N>(f); } }

struct Ptrs { const void *x, *wqkv, *wo, *wg, *wu, *wd; char* out; char* ws; };

DI srd_t mks(const void* p, int bytes) { return srd_t{(int)(size_t)p, (int)((size_t)p >> 32) & 0xffff, bytes, 0x00020000}; }
DI v4u tag4(f4 v, unsigned tag) { v4u o; for (int i = 0; i < 4; i++) o[i] = __builtin_amdgcn_perm(__float_as_uint(v[i]), tag, 0x07060500u); return o; }
DI unsigned tagx(unsigned m, v4u v, unsigned tag) { return m | ((v[0] ^ tag) | (v[1] ^ tag)) | ((v[2] ^ tag) | (v[3] ^ tag)); }
DI unsigned lo2(unsigned a, unsigned b) { return __builtin_amdgcn_perm(b, a, 0x05040100u); }
DI unsigned hi2(unsigned a, unsigned b) { return __builtin_amdgcn_perm(b, a, 0x07060302u); }
DI void wait_flags(rsrc_t ws, int off, unsigned long long lines, int l, unsigned tag, int id DBGA) {
  const int vo = ((lines >> l) & 1) ? l * 128 : OOB;
#pragma nounroll
  for (int spin = 0; spin < SPIN_CAP; spin++) {
    MEMBAR();
    const v4u f = ldf(ws, vo, off, 0);
    if ((__builtin_amdgcn_ballot_w64(((f[0] ^ tag) | (f[1] ^ tag) | (f[2] ^ tag) | (f[3] ^ tag)) != 0u) & lines) == 0) break;
    DBG(id);
    __builtin_amdgcn_s_sleep(2);
  }
}
DI void set_flag(rsrc_t ws, int off, int b, bool on, int l, unsigned tag) {
  __builtin_amdgcn_raw_buffer_store_b32(tag, ws, (on && l < 8) ? l * 8192 : OOB, off + (b >> 2) * 128 + (b & 3) * 4, SC1);
}
DI void wait_words(unsigned v, unsigned tag, srd_t srd, int voff, int soff) {
  int cnt;
  asm volatile("s_mov_b32 %1, 0x10000\n"
    "1:\n\tv_cmp_ne_u32 vcc, %0, %2\n\ts_cbranch_vccz 2f\n\t"
    "buffer_load_dword %0, %3, %4, %5 offen sc1\n\ts_waitcnt vmcnt(0)\n\t"
    "s_sub_u32 %1, %1, 1\n\ts_cbranch_scc0 1b\n"
    "2:" : "+v"(v), "=&s"(cnt) : "v"(tag), "v"(voff), "s"(srd), "s"(soff) : "vcc", "scc", "memory");
}
template <int N> DI void poll_frags(v4u (&v)[N], rsrc_t ws, int voff, int base, int id DBGA) {
#pragma nounroll
  for (int spin = 0; spin < SPIN_CAP; spin++) {
    MEMBAR();
    const int lo = fresh(voff);
    unsigned m = 0;
#pragma unroll
    for (int i = 0; i < N; i++) v[i] = ldf(ws, lo, base, i);
#pragma unroll
    for (int i = 0; i < N; i++) m = mx4(m, v[i]);
    if (!bad(m)) break;
    DBG(id);
  }
}
DI void poll71(v4u (&v)[8], rsrc_t ws, int voff, int s0, int v1, int s1, int id DBGA) {
#pragma nounroll
  for (int spin = 0; spin < SPIN_CAP; spin++) {
    MEMBAR();
    const int lo = fresh(voff);
    unsigned m = 0;
#pragma unroll
    for (int i = 0; i < 7; i++) v[i] = ldf(ws, lo, s0, i);
    v[7] = ldf(ws, v1, s1, 0);
#pragma unroll
    for (int i = 0; i < 8; i++) m = mx4(m, v[i]);
    if (!bad(m)) break;
    DBG(id);
  }
}
DI void store_if(int c, unsigned v, srd_t srd, int voff, int soff) {
  asm volatile("s_cmp_lg_u32 %0, 0\n\ts_cbranch_scc1 1f\n\tbuffer_store_dword %1, %2, %3, %4 offen sc1\n1:" :: "s"(c), "v"(v), "v"(voff), "s"(srd), "s"(soff) : "scc", "memory");
}
template <class Fn> DI void poll8(v4u (&v)[8], rsrc_t ws, int voff, int s0, int s1, int id DBGA, Fn&& hook) {
  {
    const int lo = fresh(voff);
    unsigned m = 0;
#pragma unroll
    for (int i = 0; i < 8; i++) v[i] = ldf(ws, lo, i < 4 ? s0 : s1, i & 3);
    SB(); hook(); SB();
#pragma unroll
    for (int i = 0; i < 8; i++) m = mx4(m, v[i]);
    if (!bad(m)) return;
    DBG(id);
  }
#pragma nounroll
  for (int spin = 0; spin < SPIN_CAP; spin++) {
    MEMBAR();
    const int lo = fresh(voff);
    unsigned m = 0;
#pragma unroll
    for (int i = 0; i < 8; i++) v[i] = ldf(ws, lo, i < 4 ? s0 : s1, i & 3);
#pragma unroll
    for (int i = 0; i < 8; i++) m = mx4(m, v[i]);
    if (!bad(m)) break;
    DBG(id);
  }
}
typedef short s4v __attribute__((ext_vector_type(4)));
#define LAS __attribute__((address_space(3)))
DI v4u tr_frag(const char* sm, int off) {
  const v2u a = __builtin_bit_cast(v2u, __builtin_amdgcn_ds_read_tr16_b64_v4i16((LAS s4v*)((LAS char*)sm + off)));
  const v2u b = __builtin_bit_cast(v2u, __builtin_amdgcn_ds_read_tr16_b64_v4i16((LAS s4v*)((LAS char*)sm + off + 448)));
  return v4u{a[0], a[1], b[0], b[1]};
}
DI void dma_kv(int voff, srd_t srd, int soff, int lds) {
  unsigned keep;
  asm volatile("s_mov_b32 %0, m0\n\ts_mov_b32 exec_hi, 0x00ffffff\n\ts_mov_b32 m0, %4\n\ts_nop 0\n\t"
               "buffer_load_dwordx4 %1, %2, %3 offen offset:896" WNT " lds\n\tbuffer_load_dwordx4 %1, %2, %3 offen offset:1792" WNT " lds\n\t"
               "s_mov_b32 m0, %0\n\ts_mov_b32 exec_hi, -1" : "=&s"(keep) : "v"(voff), "s"(srd), "s"(soff), "s"(lds - 896) : "memory");
}
DI void dma_q(int voff, srd_t srd, int soff, int lds) {
  unsigned keep;
  asm volatile("s_mov_b32 %0, m0\n\ts_mov_b32 exec_hi, 0x00ffffff\n\ts_mov_b32 m0, %4\n\ts_nop 0\n\t"
               "buffer_load_dwordx4 %1, %2, %3 offen" WNT " lds\n\t"
               "s_mov_b32 m0, %0\n\ts_mov_b32 exec_hi, -1" : "=&s"(keep) : "v"(voff), "s"(srd), "s"(soff), "s"(lds) : "memory");
}
DI void lds_arrive(char* sm, int off, int l) { if (l == 0) __hip_atomic_fetch_add((unsigned*)(sm + off), 1u, __ATOMIC_RELAXED, __HIP_MEMORY_SCOPE_WORKGROUP); }
DI void lds_arrive_nb(int addr_lane0, int addr_other, int l) { asm volatile("ds_add_u32 %0, %1" :: "v"(l == 0 ? addr_lane0 : addr_other + l * 4), "v"(1) : "memory"); }
DI void lds_wait(int addr, unsigned n) {
  unsigned t;
  asm volatile("1:\n\tds_read_b32 %0, %1\n\ts_waitcnt lgkmcnt(0)\n\tv_cmp_gt_u32 vcc, %2, %0\n\ts_cbranch_vccnz 1b" : "=&v"(t) : "v"(addr), "s"(n) : "vcc", "memory");
}
DI void dma_full_nt(int voff, srd_t srd, int soff, int lds) {
  unsigned keep;
  asm volatile("s_mov_b32 %0, m0\n\ts_mov_b32 m0, %4\n\ts_nop 0\n\tbuffer_load_dwordx4 %1, %2, %3 offen nt lds\n\ts_mov_b32 m0, %0" : "=&s"(keep) : "v"(voff), "s"(srd), "s"(soff), "s"(lds) : "memory");
}
DI void dma_full(int voff, srd_t srd, int soff, int lds) {
  unsigned keep;
  asm volatile("s_mov_b32 %0, m0\n\ts_mov_b32 m0, %4\n\ts_nop 0\n\tbuffer_load_dwordx4 %1, %2, %3 offen lds\n\ts_mov_b32 m0, %0" : "=&s"(keep) : "v"(voff), "s"(srd), "s"(soff), "s"(lds) : "memory");
}
DI v2u tr_half(const char* sm, int off) { return __builtin_bit_cast(v2u, __builtin_amdgcn_ds_read_tr16_b64_v4i16((LAS s4v*)((LAS char*)sm + off))); }
template <int G> DI v4u red_final(const char* sm, int off, v4u res) {
  f4 e = {bflo(res[0]), bflo(res[1]), bflo(res[2]), bflo(res[3])}, o = {bfhi(res[0]), bfhi(res[1]), bfhi(res[2]), bfhi(res[3])};
#pragma unroll
  for (int g = 0; g < 16; g += G) {
    v4u t[G];
#pragma unroll
    for (int i = 0; i < G; i++) t[i] = lds_rd(sm, off + (g + i) * 4096);
    SB();
#pragma unroll
    for (int w = 0; w < G / 2; w++) { e += __builtin_bit_cast(f4, t[2 * w]); o += __builtin_bit_cast(f4, t[2 * w + 1]); }
    SB();
  }
  v4u out;
#pragma unroll
  for (int i = 0; i < 4; i++) out[i] = pk2(e[i], o[i]);
  return out;
}

constexpr int L_X = 0;
constexpr int L_KV = 57344;
constexpr int L_KVIN = 114688;
constexpr int L_WO = 57344;
constexpr int L_OB0 = 0, L_OB1 = 114688;
constexpr int L_H0 = 0;
constexpr int L_H1 = 65536;
constexpr int L_HST = 131328;
constexpr int L_XN = 149504;
constexpr int L_MISC = 157696;
constexpr int L_DUMMY = 157824;
constexpr int L_BYTES = 158080;

#define ABL 0
#define DLY_X 20
#define DLY_AV 10
#define AV2AT 14
#define WARM 0
#define ROTD 0
#define TFIRST 0
#define UPOS 0
#define UNT " nt"
#define DLY_KQ 4
#define J2AT 9
#define FAAT 23
#define NSETS 1
#define OAUX 16
#define ROTC 0
#define DLY_PQ 0
#define DLY_KF 0
#define DLY_F1 8
#define DLY_KV 24
#define NPF0 6
#define NPF1 14
#define NPF2 9
#define NPF3 15
#define NWD0 8

__global__ __launch_bounds__(512, 2) void fused_block(Ptrs p) {
  __shared__ __attribute__((aligned(16))) char sm[L_BYTES];
  STAMP(0);
  const int b = blockIdx.x, xcd = b & 7, idx = b >> 3;
  const int w = __builtin_amdgcn_readfirstlane(threadIdx.x >> 6), l = threadIdx.x & 63, l16 = l * 16;
  unsigned ep, ut[8];
  {
    unsigned long long pc; int len;
    asm volatile("s_getpc_b64 %0\n.Lfb_pc:\n\ts_mov_b32 %1, .Lfb_end-.Lfb_pc" : "=s"(pc), "=s"(len));
    const void* eb = w == 0 ? (const void*)p.ws : (w == 1 ? p.x : (w == 2 ? p.wqkv : (w == 3 ? p.wo : (const void*)pc)));
    const int en = w == 0 ? 64 : (w == 4 ? len : (!TFIRST || w > 4 ? 0 : (w == 1 ? S * D * 2 : (w == 2 ? D * 3 * D * 2 : D * D * 2))));
    const int nl = w == 1 ? 56 : (w == 2 ? 37 : (w == 3 ? 13 : 32)), e0 = (w == 4 ? (idx & 7) : b) * nl;
    asm volatile("buffer_load_dword %0, %1, %2, 0 offen sc1" : "=v"(ep) : "v"(w == 0 ? 0 : (l < nl ? (e0 + l) * 128 : OOB)), "s"(mks(eb, en)) : "memory");
  }
  const int uks = 7 * ((b >> 1) & 3) + (w ? w - 1 : 0);
  asm volatile("s_barrier" ::: "memory");
  {
    const srd_t us = mks((b & 1) ? p.wu : p.wg, D * F * 2);
    const int uv = (b < 224 && w) ? (l >> 5) * 28672 + (l & 31) * 4 : OOB, uo = uks * 57344 + idx * 128;
#pragma unroll
    for (int j = 0; j < 8; j++) asm volatile("buffer_load_dword %0, %1, %2, %3 offen" UNT : "=v"(ut[j]) : "v"(uv), "s"(us), "s"(uo + j * 3584) : "memory");
  }
  const int t64 = 8 * (xcd & 3) + (idx & 7), hp = xcd < 4 ? 3 - (idx >> 3) : 4 + (idx >> 3);
  const int rot = wrap((idx & 7) + 2 * (idx >> 3), 7) % 7;
  v4u xg[7];
  {
    const srd_t xs = mks(p.x, S * D * 2);
    const int xv = (l >> 3) * 896 + (l & 7) * 16, xso = (t64 * 64 + w * 8) * 896;
#pragma unroll
    for (int i = 0; i < 7; i++) asm volatile("buffer_load_dwordx4 %0, %1, %2, %3 offen" XNT : "=v"(xg[i]) : "v"(xv), "s"(xs), "s"(xso + wrap(i + rot, 7) * 128) : "memory");
  }
  asm volatile("s_barrier" ::: "memory");
  const srd_t wsrd = mks(p.wqkv, D * 3 * D * 2);
  const int l7 = (l * 37) >> 8, wv = l7 * 2688 + (l - 7 * l7) * 16 + hp * 112;
  const int lbase = (int)(size_t)(LAS char*)sm;
#define DMA_KV(c) dma_kv(wv, wsrd, (wrap((c) + rot, 7) * 8 + w) * 21504, lbase + L_KV + ((c) * 8 + w) * 1792)
#define DMA_Q(c) dma_q(wv, wsrd, (wrap((c) + rot, 7) * 8 + w) * 21504, lbase + L_X + ((c) * 8 + w) * 896)
  const int th2 = idx >= 14, tB = 2 * xcd + th2, m = idx - 14 * th2, T = m >> 1, a = m & 1;
  const bool ffn = idx < 28;
  const int r = l & 31, c16 = l & 15, g4 = l >> 4, hh = l >> 5;
  const v4u sent = {SENT, SENT, SENT, SENT};
  const int tt = w & 1, hd = (w >> 1) & 1;
  const int pq_off = OFF_PQ + ((2 * t64 + tt) * 16 + 2 * hp + hd) * 2048 + l16;
  const int part_off = OFF_PART + ((2 * hp + hd) * 32 + t64) * 4096 + (w & 1) * 2048 + l16;
  DMA_KV(0);
  SB();
  const int ai = idx, tC = 4 * (xcd & 3) + (ai & 3), cC = 7 * (xcd >> 2) + (ai >> 2);
  const int jcs = b - 224;
  *(v4u*)(sm + L_MISC + (threadIdx.x == 0 ? 0 : (threadIdx.x == 1 ? 32 : 16))) = v4u{0u, 0u, 0u, 0u};
  SB();
  asm volatile("s_waitcnt vmcnt(%8)" : "+v"(ep), "+v"(xg[0]), "+v"(xg[1]), "+v"(xg[2]), "+v"(xg[3]), "+v"(xg[4]), "+v"(xg[5]), "+v"(xg[6]) : "i"(UPOS == 1 ? 10 : 2));
  STAMP(1);
  DMA_KV(1);
  {
    float s = 0.f, ss = 0.f;
#pragma unroll
    for (int i = 0; i < 7; i++)
#pragma unroll
      for (int j = 0; j < 4; j++) { s = dot2(xg[i][j], ONES, s); ss = dot2(xg[i][j], xg[i][j], ss); }
    SB(); DMA_KV(2); SB();
    s = dpp_add<0x141>(dpp_add<0x4E>(dpp_add<0xB1>(s))); ss = dpp_add<0x141>(dpp_add<0x4E>(dpp_add<0xB1>(ss)));
    const float mean = s * (1.f / D), var = fmaxf(ss * (1.f / D) - mean * mean, 0.f);
    const float ra = __builtin_amdgcn_rsqf(var + 1e-5f), rb = -mean * ra;
    const int o = L_X + w * 7168 + (l >> 3) * 128 + (((l & 7) ^ (4 * (w & 1) + (l >> 4))) * 16);
#pragma unroll
    for (int i = 0; i < 7; i++) {
      float f[8];
#pragma unroll
      for (int j = 0; j < 4; j++) { f[2 * j] = bflo(xg[i][j]); f[2 * j + 1] = bfhi(xg[i][j]); }
#pragma unroll
      for (int j = 0; j < 8; j++) { f[j] = __builtin_fmaf(f[j], ra, rb); asm volatile("" : "+v"(f[j])); }
      const v4u xn = {pk2(f[0], f[1]), pk2(f[2], f[3]), pk2(f[4], f[5]), pk2(f[6], f[7])};
      lds_wr(sm, o + i * 1024, xn);
      if (i == 3) { SB(); DMA_KV(3); SB(); }
    }
  }
  *(unsigned*)(sm + L_MISC + (threadIdx.x == 0 ? 0 : 16)) = ep + 1u;
  SB();
  DMA_KV(4);
  BAR();
  STAMP(2);
  const unsigned tag = __builtin_amdgcn_readfirstlane(*(const unsigned*)(sm + L_MISC));
  char* const wsb = p.ws + (size_t)(tag & (NSETS - 1)) * SET_BYTES;
  const rsrc_t ws = mk(wsb, WS_BYTES);
  const srd_t srd = mks(wsb, WS_BYTES);
  SB();
  v4u xf[28];
  {
    const int row = 32 * tt + r, xrb = L_X + (row >> 3) * 7168 + (row & 7) * 128, sw = (row >> 1) & 7;
    int xa[4];
#pragma unroll
    for (int i = 0; i < 4; i++) xa[i] = xrb + (((2 * i + hh) ^ sw) * 16);
#pragma unroll
    for (int i = 0; i < 28; i++) xf[i] = lds_rd(sm, xa[i & 3] + (i >> 2) * 1024);
  }
  const int trl = (c16 >> 2) * 112 + hd * 56 + min(4 * (g4 & 1) + (c16 & 3), 6) * 8;
  f16v acc = {};
  const int ba = fresh(L_KV + (w >> 2) * 896 + hh * 1792 + trl), bb2 = fresh(L_KV + (w >> 2) * 896 + hh * 1792 + trl + 57344);
#define RD_KV(i) tr_frag(sm, (i) < 16 ? ba + (i) * 3584 : bb2 + ((i) - 16) * 3584)
  v4u bb[4];
  {
    SB();
    {
      asm volatile("s_waitcnt vmcnt(8)" : "+v"(ut[0]), "+v"(ut[1]), "+v"(ut[2]), "+v"(ut[3]), "+v"(ut[4]), "+v"(ut[5]), "+v"(ut[6]), "+v"(ut[7]));
      const int d0 = w ? (b < 224 ? OFF_WC + (116 * idx + uks) * 1024 + (g4 & 1) * 59392 + hh * 512 + (b & 1) * 256 + c16 * 16 : OOB) : (ffn ? OOB : l16 + OFF_KVF + jcs * 1024);
      const int d1 = w ? d0 + 29696 : ((ffn && l < 32) ? l16 + OFF_HS + ((tB * 8 + T) * 2 + a) * 512 : OOB);
      const unsigned f_ = w ? 0u : SENT;
      stc(v4u{lo2(ut[0], ut[1]), lo2(ut[2], ut[3]), lo2(ut[4], ut[5]), lo2(ut[6], ut[7])} | f_, ws, d0);
      stc(v4u{hi2(ut[0], ut[1]), hi2(ut[2], ut[3]), hi2(ut[4], ut[5]), hi2(ut[6], ut[7])} | f_, ws, d1);
      SB(); DMA_KV(5); SB();
      stc(sent, ws, pq_off + (w >> 2) * 1024);
      stc(sent, ws, ffn ? l16 + OFF_HF + ((4 * tB + a + 2 * (w >> 2)) * 28 + 4 * T + (w & 3)) * 1024 : OOB);
      SB(); DMA_KV(6);
    }
    SB(); asm volatile("s_waitcnt vmcnt(10) lgkmcnt(0)\n\ts_barrier" ::: "memory"); SB();
#pragma unroll
    for (int i = 0; i < 3; i++) bb[i] = RD_KV(i);
    sfor<0, 13>([&](auto II) {
      constexpr int i = decltype(II)::v;
      if constexpr (i % 7 == 0) __builtin_amdgcn_s_setprio(3 - i / 7);
      if constexpr (i == 4 || i == 8 || i == 12) DMA_Q(i / 4 - 1);
      bb[(i + 3) & 3] = RD_KV(i + 3);
      acc = mfma32(xf[i], bb[i & 3], acc);
      SB();
    });
    asm volatile("s_waitcnt vmcnt(3) lgkmcnt(0)\n\ts_barrier" ::: "memory"); SB();
    STAMP(3);
  }
  v4u wc[29];
  const int wc_off = OFF_WC + (8 * cC + w) * 29 * 1024;
#define LD_WC(i) wc[i] = ldf(ws, lo, wc_off, i)
  const srd_t os = mks(ffn ? p.wo : p.wd, ffn ? D * D * 2 : F * D * 2);
  const int ov = ffn ? (l >> 3) * 896 + (((l & 7) ^ (2 * ((l >> 4) & 3))) * 16) + 128 * T : (jcs < 28 ? l16 + jcs * 57344 : OOB), og = ffn ? 14 * (2 * th2 + a) : 0, ost = ffn ? 7168 : 1024;
#define DMA_WO(g) do { const int gg_ = wrap((g) + og, 56); dma_full(ov, os, gg_ * ost, lbase + L_WO + gg_ * 1024); } while (0)
  if (w < 4) {
    __builtin_amdgcn_s_setprio(3);
    sfor<13, 28>([&](auto II) {
      constexpr int i = decltype(II)::v;
      if constexpr (i == 21) { SB(); lds_arrive_nb(lbase + L_MISC + 32, lbase + L_DUMMY, l); SB(); }
      if constexpr (i + 3 < 28) bb[(i + 3) & 3] = RD_KV(i + 3);
      acc = mfma32(xf[i], bb[i & 3], acc);
      SB();
    });
    STAMP(4);
    {
      v4u fr[2];
#pragma unroll
      for (int j = 0; j < 8; j++) fr[j >> 2][j & 3] = pk2(phi(acc[2 * j]), phi(acc[2 * j + 1]));
      SB(); lds_wait(lbase + L_MISC + 32, 8u);
      lds_wr(sm, L_KVIN + w * 2048 + l16, fr[0]); lds_wr(sm, L_KVIN + w * 2048 + 1024 + l16, fr[1]);
    }
    SB(); asm volatile("s_waitcnt vmcnt(0) lgkmcnt(0)\n\ts_barrier" ::: "memory"); SB();
    STAMP(5);
    {
      v4u ka[2], vb[2][2];
      const int ko = L_KVIN + hd * 4096 + g4 * 512 + c16 * 16;
#pragma unroll
      for (int t_ = 0; t_ < 2; t_++) { ka[t_] = lds_rd(sm, ko + t_ * 2048 + (w & 1) * 256); vb[t_][0] = lds_rd(sm, ko + 8192 + t_ * 2048); vb[t_][1] = lds_rd(sm, ko + 8192 + t_ * 2048 + 256); }
      SB();
      f4 kv[2] = {};
#pragma unroll
      for (int t_ = 0; t_ < 2; t_++) { kv[0] = mfma16(ka[t_], vb[t_][0], kv[0]); kv[1] = mfma16(ka[t_], vb[t_][1], kv[1]); }
      stc(tag4(kv[0], tag), ws, part_off); stc(tag4(kv[1], tag), ws, part_off + 1024);
    }
    STAMP(6);
    if (w == 3) {
      wait_flags(ws, OFF_FS + xcd * 8192, ~0ull, l, tag, 0 DBGP);
      if (b == 0 && l == 0) __builtin_amdgcn_raw_buffer_store_b32(tag, mk(p.ws, 64), 0, OFF_EPOCH, SC1);
      lds_arrive(sm, L_MISC + 4, l);
    } else if (ffn) {
      __builtin_amdgcn_s_setprio(0);
#pragma nounroll
      for (int g = w; g < 56; g += 3) DMA_WO(g);
    }
    __builtin_amdgcn_s_setprio(0);
  } else {
    __builtin_amdgcn_s_setprio(0);
    sfor<13, 28>([&](auto II) {
      constexpr int i = decltype(II)::v;
      if constexpr (i == 13) store_if(w - 7, tag, srd, l < 8 ? l * 8192 : OOB, OFF_FS + (b >> 2) * 128 + (b & 3) * 4);
      if constexpr (i == 21) { SB(); lds_arrive_nb(lbase + L_MISC + 32, lbase + L_DUMMY, l); SB(); }
      if constexpr (i == 15 || i == 18 || i == 22 || i == 25) {
        constexpr int c = i == 15 ? 3 : (i == 18 ? 4 : (i == 22 ? 5 : 6));
        dma_q(wv, wsrd, (wrap(c + rot, 7) * 8 + w - 4) * 21504, lbase + L_X + (c * 8 + w - 4) * 896); DMA_Q(c);
      }
      if constexpr (i + 3 < 28) bb[(i + 3) & 3] = RD_KV(i + 3);
      acc = mfma32(xf[i], bb[i & 3], acc);
      SB();
    });
    STAMP(4);
    {
      const unsigned pad = r == 28 ? ONES : 0u;
      v4u fr[2];
#pragma unroll
      for (int j = 0; j < 8; j++) { const unsigned t_ = pk2(acc[2 * j], acc[2 * j + 1]); fr[j >> 2][j & 3] = r < 28 ? t_ : pad; }
      SB(); lds_wait(lbase + L_MISC + 32, 8u);
      lds_wr(sm, L_KVIN + w * 2048 + l16, fr[0]); lds_wr(sm, L_KVIN + w * 2048 + 1024 + l16, fr[1]);
    }
    BAR();
    STAMP(5);
    asm volatile("s_waitcnt vmcnt(0)" ::: "memory");
    lds_arrive(sm, L_MISC + 36, l);
    lds_wait(lbase + L_MISC + 36, 4u);
    const int qa = fresh(L_X + hh * 896 + trl);
    f16v aq = {};
#pragma unroll
    for (int i = 0; i < 3; i++) bb[i] = tr_frag(sm, qa + i * 1792);
    SB();
    sfor<0, 28>([&](auto II) {
      constexpr int i = decltype(II)::v;
      if constexpr (i + 3 < 28) bb[(i + 3) & 3] = tr_frag(sm, qa + (i + 3) * 1792);
      aq = mfma32(bb[i & 3], xf[i], aq);
      SB();
    });
    __builtin_amdgcn_s_setprio(3);
#pragma unroll
    for (int s = 0; s < 2; s++) {
      v4u pf = {pk2(phi(aq[8 * s]), phi(aq[8 * s + 1])), pk2(phi(aq[8 * s + 2]), phi(aq[8 * s + 3])), pk2(phi(aq[8 * s + 4]), phi(aq[8 * s + 5])), pk2(phi(aq[8 * s + 6]), phi(aq[8 * s + 7]))};
      if (s == 1) { pf[2] = hh ? 0u : pf[2]; pf[3] = hh ? 0u : pf[3]; }
      stc(canon(pf), ws, pq_off + s * 1024);
    }
    __builtin_amdgcn_s_setprio(0);
    STAMP(8);
  }
  SB();
  STAMP(10);
  if (!ffn) {
    {
      const int rh = jcs >> 1, rj = (jcs & 1) * 8 + w;
      v4u pv[8];
      const int so = OFF_PART + rh * 131072 + ((rj >> 3) * 2 + ((rj >> 1) & 1)) * 1024 + ((rj >> 2) & 1) * 256 + (rj & 1) * 128;
      const unsigned tb = tag & 0xffu;
      if (w < 4) __builtin_amdgcn_s_sleep(DLY_KV); else __builtin_amdgcn_s_sleep(DLY_KQ);
#pragma nounroll
      for (int spin = 0; spin < SPIN_CAP; spin++) {
        MEMBAR();
        const int vo = fresh(g4 * 4096 + (c16 >> 3) * 512 + (c16 & 7) * 16);
        unsigned mm = 0;
#pragma unroll
        for (int i = 0; i < 8; i++) pv[i] = ldf(ws, vo, so + i * 16384, 0);
#pragma unroll
        for (int i = 0; i < 8; i++) { pv[i] ^= tb; mm |= (pv[i][0] | pv[i][1]) | (pv[i][2] | pv[i][3]); }
        if (__builtin_amdgcn_ballot_w64((mm & 0xffu) != 0u) == 0) break;
        DBG(3);
      }
      f4 s = __builtin_bit_cast(f4, pv[0]);
#pragma unroll
      for (int i = 1; i < 8; i++) s += __builtin_bit_cast(f4, pv[i]);
#pragma unroll
      for (int i = 0; i < 4; i++) s[i] = sum32(sum16(s[i]));
      const v2u o = {min(pk2(s[0], s[1]), SENT - 1u), min(pk2(s[2], s[3]), SENT - 1u)};
      __builtin_amdgcn_raw_buffer_store_b64(o, ws, l < 16 ? (c16 & 7) * 16 + (c16 >> 3) * 8 : OOB, OFF_KVF + jcs * 1024 + w * 128, SC1);
      STAMP(9);
      BAR();
    }
    if (jcs < 28) {
#pragma unroll
      for (int i = 0; i < 7; i++) dma_full_nt(ov, os, (8 * i + w) * 1024, lbase + L_WO + (8 * i + w) * 1024);
      const int ft = 4 * jcs + (w >> 1), lo = fresh(l16);
      v4u v[14]; float cs = 0.f;
#pragma unroll
      for (int i = 0; i < 14; i++) v[i] = ldf(ws, lo, OFF_WC + (ft * 29 + 14 * (w & 1)) * 1024, i);
#pragma unroll
      for (int i = 0; i < 14; i++)
#pragma unroll
        for (int j = 0; j < 4; j++) cs = dot2(v[i][j], ONES, cs);
      cs = sum32(cs);
      *(float*)(sm + L_HST + w * 256 + l * 4) = cs;
      BAR();
      if (w < 7) {
        unsigned u_[4][8];
        const int dv = fresh(L_WO + hh * 7168 + r * 4 + 128 * w);
#pragma unroll
        for (int u = 0; u < 4; u++)
#pragma unroll
          for (int j = 0; j < 8; j++) u_[u][j] = *(const unsigned*)(sm + dv + (u & 1) * 896 + (u >> 1) * 28672 + (j & 3) * 1792 + (j >> 2) * 14336);
        SB();
#pragma unroll
        for (int u = 0; u < 4; u++) {
          const int d0 = OFF_WD + (224 * w + 4 * jcs + u) * 1024 + l16;
          stc(v4u{lo2(u_[u][0], u_[u][1]), lo2(u_[u][2], u_[u][3]), lo2(u_[u][4], u_[u][5]), lo2(u_[u][6], u_[u][7])}, ws, d0);
          stc(v4u{hi2(u_[u][0], u_[u][1]), hi2(u_[u][2], u_[u][3]), hi2(u_[u][4], u_[u][5]), hi2(u_[u][6], u_[u][7])}, ws, d0 + 114688);
        }
      }
      if (!(w & 1)) {
        cs += *(const float*)(sm + L_HST + (w + 1) * 256 + l * 4);
        const unsigned hi = pk2(cs, cs), lw = pk2(cs - bflo(hi), 0.f);
        const v4u fr = {l < 32 ? hi : 0u, l < 32 ? lw : 0u, 0u, 0u};
        stc(fr, ws, l16 + OFF_WC + (ft * 29 + 28) * 1024);
      }
      asm volatile("s_waitcnt vmcnt(0)" ::: "memory");
      BAR();
    }
    if (w == 0) set_flag(ws, OFF_FD, b, true, l, tag);
  } else {
    const int c4 = w & 3, th = w >> 2;
    v4u wo[16], pq[8], kvf[4], hv[8];
    int woa[2];
#pragma unroll
    for (int e = 0; e < 2; e++) woa[e] = L_WO + (16 * (g4 >> 1) + 4 * (g4 & 1) + (c16 >> 2)) * 128 + (((2 * c4 + ((c16 >> 1) & 1)) ^ (2 * ((((e + g4) & 1) << 1) + (c16 >> 3)))) * 16) + (c16 & 1) * 8;
    const int wo8 = g4 == 3 ? 0 : 1024;
    const int hoff = OFF_HF + (4 * tC * 28 + 7 * w) * 1024;
    v2u xr[2];
    lds_wait(lbase + L_MISC + 4, 1u);
    {
      const int lo = fresh(l16);
#pragma unroll
      for (int ps = 0; ps < 2; ps++) xr[ps] = *(const v2u*)((const char*)p.x + (size_t)((4 * tB + a + 2 * ps) * 32 + 16 * th + c16) * 896 + (64 * T + 16 * c4 + 4 * g4) * 2);
#pragma unroll
      for (int i = 0; i < NPF0; i++) LD_WC(i);
      SB();
      asm volatile("s_waitcnt vmcnt(%0)" :: "i"(NPF0 + 2) : "memory");
      lds_arrive(sm, L_MISC + 8, l);
    }
    lds_wait(lbase + L_MISC + 8, 8u);
    __builtin_amdgcn_s_setprio(2);
    poll8(pq, ws, l16, OFF_PQ + ((4 * tB + a) * 32 + 4 * w) * 1024, OFF_PQ + ((4 * tB + a + 2) * 32 + 4 * w) * 1024, 4 DBGP, [&] {
      const int lo = fresh(l16);
      __builtin_amdgcn_s_setprio(0);
#pragma unroll
      for (int i = 0; i < 16; i++) {
        const v2u x0 = tr_half(sm, woa[i & 1] + i * 3584), x1 = tr_half(sm, woa[i & 1] + wo8 + i * 3584);
        wo[i] = v4u{x0[0], x0[1], x1[0], x1[1]};
      }
      SB();
      if (DLY_KF) __builtin_amdgcn_s_sleep(DLY_KF);
      __builtin_amdgcn_s_setprio(3);
#pragma unroll
      for (int i = 0; i < 4; i++) kvf[i] = ldf(ws, lo, OFF_KVF + 4 * w * 1024, i);
      __builtin_amdgcn_s_setprio(0);
      SB();
#pragma unroll
      for (int i = NPF0; i < NPF1; i++) LD_WC(i);
    });
    STAMP(7);
    {
      unsigned mm = 0;
#pragma unroll
      for (int i = 0; i < 4; i++) mm = mx4(mm, kvf[i]);
      if (bad(mm)) { DBG(5); __builtin_amdgcn_s_setprio(3); poll_frags<4>(kvf, ws, l16, OFF_KVF + 4 * w * 1024, 5 DBGP); __builtin_amdgcn_s_setprio(0); }
    }
    STAMP(11);
    sfor<0, 2>([&](auto PS) {
      constexpr int ps = decltype(PS)::v;
      const int lo = fresh(l16);
      f16v n[2] = {};
#pragma unroll
      for (int s = 0; s < 2; s++)
#pragma unroll
        for (int hd = 0; hd < 2; hd++) n[hd] = mfma32(kvf[2 * hd + s], pq[4 * ps + 2 * hd + s], n[hd]);
      constexpr int pa = ps ? NPF1 + 4 : NPF1;
      LD_WC(pa); LD_WC(pa + 1);
      SB();
#pragma unroll
      for (int hd = 0; hd < 2; hd++) {
        const auto sw = __builtin_amdgcn_permlane32_swap(__float_as_uint(n[hd][12]), __float_as_uint(n[hd][12]), false, false);
        const float inv = __builtin_amdgcn_rcpf(__uint_as_float(sw[1]) + 1e-6f);
        v4u o0, o1;
#pragma unroll
        for (int i = 0; i < 4; i++) { o0[i] = pk2(n[hd][2 * i] * inv, n[hd][2 * i + 1] * inv); o1[i] = pk2(n[hd][8 + 2 * i] * inv, n[hd][9 + 2 * i] * inv); }
        o1[2] = hh ? 0u : o1[2];
        lds_wr(sm, (ps ? L_OB1 : L_OB0) + (4 * w + 2 * hd) * 1024 + lo, o0);
        lds_wr(sm, (ps ? L_OB1 : L_OB0) + (4 * w + 2 * hd + 1) * 1024 + lo, o1);
      }
      BAR();
      if (ps == 0) STAMP(12);
      v4u ob[8];
      const int oo = fresh((ps ? L_OB1 : L_OB0) + g4 * 512 + th * 256 + c16 * 16), vh = fresh(l16);
#pragma unroll
      for (int i = 0; i < 8; i++) ob[i] = lds_rd(sm, oo + i * 2048);
      f4 hacc = {bflo(xr[ps][0]), bfhi(xr[ps][0]), bflo(xr[ps][1]), bfhi(xr[ps][1])};
      SB();
#pragma unroll
      for (int i = 0; i < 16; i++) {
        hacc = mfma16(wo[i], ob[i & 7], hacc);
        if (i < 8) ob[i] = lds_rd(sm, oo + (8 + i) * 2048);
        if (ps == 1 && (i & 1) && i < 14) hv[i >> 1] = ldf(ws, vh, hoff, i >> 1);
        SB();
      }
      const unsigned p0 = min(pk2(hacc[0], hacc[1]), SENT - 1u), p1 = min(pk2(hacc[2], hacc[3]), SENT - 1u);
      const auto s0 = __builtin_amdgcn_permlane16_swap(p0, p0, false, false), s1 = __builtin_amdgcn_permlane16_swap(p1, p1, false, false);
      const v4u hf = {s0[0], s1[0], s0[1], s1[1]};
      stc(hf, ws, (g4 & 1) ? OOB : OFF_HF + ((4 * tB + a + 2 * ps) * 28 + 4 * T + c4) * 1024 + (g4 >> 1) * 512 + th * 256 + c16 * 16);
      STAMP(13 + ps);
      LD_WC(pa + 2); LD_WC(pa + 3);
      {
        const float s_ = dot2(p0, ONES, dot2(p1, ONES, 0.f)), ss_ = dot2(p0, p0, dot2(p1, p1, 0.f));
        *(f2*)(sm + L_XN + ((c4 * 4 + g4) * 32 + 16 * th + c16) * 16 + ps * 8) = f2{s_, ss_};
      }
      SB();
    });
    v4u wd[28], av[14];
    const int rd = ROTD ? 2 * wrap(wrap(T + 2 * (2 * th2 + a), 7), 7) : 0;
    const int wd_off = OFF_WD + ((2 * T) * 112 + 14 * w) * 1024;
#define LD_WD(i) wd[i] = ldf(ws, (ABL & 1) ? OOB : lo, wd_off + (((i) & 1) * 112 + wrap((((i) >> 2) << 1) + rd, 14)) * 1024, ((i) >> 1) & 1)
#define LD_AV(i, off) av[i] = ldf(ws, (ABL & 4) ? OOB : lo, (off) + wrap(((i) & ~1) + rd, 14) * 1024, (i) & 1)
    const int vs = w < 7 ? l16 : OOB, hso = OFF_HS + tC * 8192 + w * 1024;
    float c1[4], vv[4]; unsigned mneg[4];
    f4 sq[7];
    auto stats_read = [&] {
#pragma unroll
      for (int i = 0; i < 7; i++) sq[i] = *(const f4*)(sm + L_HST + i * 1024 + l16);
    };
    auto stats_math = [&](int ps) {
      float s_ = sq[0][2 * ps], ss_ = sq[0][2 * ps + 1];
#pragma unroll
      for (int i = 1; i < 7; i++) { s_ += sq[i][2 * ps]; PIN(s_); ss_ += sq[i][2 * ps + 1]; }
      const float nm = s_ * (-1.f / D), ve = fmaxf(ss_ * (1.f / D) - nm * nm, 0.f) + 1e-5f;
      const float cc = __builtin_amdgcn_rsqf(ve) * -1.44269504089f;
      const unsigned mh = pk2(nm, 0.f), mm = pk2(nm, nm - bflo(mh));
      const auto s1 = __builtin_amdgcn_permlane32_swap(__float_as_uint(cc), __float_as_uint(cc), false, false);
      const auto s2 = __builtin_amdgcn_permlane32_swap(__float_as_uint(ve), __float_as_uint(ve), false, false);
      const auto s3 = __builtin_amdgcn_permlane32_swap(mm, mm, false, false);
#pragma unroll
      for (int k = 0; k < 2; k++) { c1[2 * ps + k] = __uint_as_float(s1[k]); vv[2 * ps + k] = __uint_as_float(s2[k]); mneg[2 * ps + k] = s3[k]; }
    };
    lds_arrive(sm, L_MISC + 40, l);
    {
      unsigned mm = 0;
#pragma unroll
      for (int i = 0; i < 7; i++) { lds_wr(sm, L_H0 + l16 + (7 * w + i) * 1024, hv[i]); mm = mx4(mm, hv[i]); }
      if (bad(mm)) {
        DBG(6);
        poll71(hv, ws, l16, hoff, OOB, 0, 6 DBGP);
#pragma unroll
        for (int i = 0; i < 7; i++) lds_wr(sm, L_H0 + l16 + (7 * w + i) * 1024, hv[i]);
      }
    }
    STAMP(15);
    if (w == 0) {
      lds_wait(lbase + L_MISC + 40, 8u);
      f4 t = {};
#pragma unroll
      for (int i = 0; i < 8; i++) t += *(const f4*)(sm + L_XN + i * 1024 + l16);
#pragma unroll
      for (int i = 0; i < 4; i++) t[i] = sum32(t[i]);
      stc(canon(__builtin_bit_cast(v4u, t)), ws, l < 32 ? l16 + OFF_HS + ((tB * 8 + T) * 2 + a) * 512 : OOB);
    }
    BAR();
    STAMP(16);
    const int ftt = 4 * tB + a + 2 * (w >> 2);
    v4u res;
    const int a0off = OFF_ACT + ((4 * tB + a) * 112 + 14 * w) * 1024;
    const int fdo = (56 + (r >> 2)) * 128 + (l & 3) * 4;
    f16v ac[2][2] = {};
    float eo[2]; v4u af;
    auto epi = [&](auto PS, auto EE) {
      constexpr int ps = decltype(PS)::v, e = decltype(EE)::v, k = e >> 3, j = e & 7;
      float g = ac[ps][k][j], t = g * c1[2 * ps + k]; PIN(t);
      float gu = g * ac[ps][k][8 + j]; PIN(gu);
      eo[j & 1] = gu * __builtin_amdgcn_rcpf(__builtin_fmaf(__builtin_amdgcn_exp2f(t), vv[2 * ps + k], vv[2 * ps + k]));
      PIN(eo[j & 1]);
      if constexpr (j & 1) af[j >> 1] = min(pk2(eo[0], eo[1]), SENT - 1u);
      if constexpr (j == 7) stc(af, ws, l16 + OFF_ACT + ((4 * tC + 2 * ps + k) * 112 + 8 * cC + w) * 1024);
    };
    auto fold_mean = [&](int ps) {
#pragma unroll
      for (int k = 0; k < 2; k++) { const v4u bx = {mneg[2 * ps + k], mneg[2 * ps + k] & 0xffffu, 0u, 0u}; ac[ps][k] = mfma32(wc[28], bx, ac[ps][k]); }
    };
    sfor<0, 2>([&](auto PS) {
      constexpr int ps = decltype(PS)::v;
      const int lo = fresh(l16);
      const int hb = fresh((ps ? L_H1 : L_H0) + l16);
      v4u bb[3][2];
      unsigned fa = 0, mm = 0;
      const int h1w = fresh(L_H1 + 7 * w * 1024 + l16), hsw = L_HST + w * 1024 + l16;
#pragma unroll
      for (int i = 0; i < 2; i++) { bb[i][0] = lds_rd(sm, hb + i * 1024); bb[i][1] = lds_rd(sm, hb + (28 + i) * 1024); }
      if constexpr (ps == 1) { fold_mean(0); fold_mean(1); }
      SB();
      sfor<0, 28>([&](auto II) {
        constexpr int i = decltype(II)::v;
        if constexpr (i % 7 == 0) __builtin_amdgcn_s_setprio(3 - i / 7);
        if constexpr (i % 4 == 0) STAMPX(1 + ps, 1 + i / 4);
        sfor<0, 2>([&](auto KK) {
          constexpr int k = decltype(KK)::v;
          if constexpr (i + 2 < 28) bb[(i + 2) % 3][k] = lds_rd(sm, hb + (k * 28 + i + 2) * 1024);
          ac[ps][k] = mfma32(wc[i], bb[i % 3][k], ac[ps][k]); PIN(ac[ps][k]);
          if constexpr (ps == 0 && k == 0 && i >= 15 && i < 23) {
            constexpr int f = i - 15;
            mm = mx4(mm, hv[f]);
            lds_wr(sm, f < 7 ? h1w + f * 1024 : hsw, hv[f]);
          }
          if constexpr (ps == 1 && i >= 1 && i < 5) { epi(IC<0>{}, IC<4 * (i - 1) + 2 * k>{}); epi(IC<0>{}, IC<4 * (i - 1) + 2 * k + 1>{}); }
          SB();
        });
        if constexpr (ps == 0) {
          if constexpr (NPF1 + 8 + 2 * i < 28) { LD_WC(NPF1 + 8 + 2 * i); LD_WC(NPF1 + 9 + 2 * i); }
          if constexpr (i >= 6 && i < 13) hv[i - 6] = ldf(ws, lo, hoff + 56 * 1024, i - 6);
          if constexpr (i == 13) hv[7] = ldf(ws, vs, hso, 0);
          if constexpr (i == 14) fa = __builtin_amdgcn_raw_buffer_load_b32(ws, fdo, OFF_FD + xcd * 8192, SC1);
          if constexpr (i == 27 - NWD0) { if (__builtin_amdgcn_ballot_w64(fa != tag) != 0) DBG(2); wait_words(fa, tag, srd, fdo, OFF_FD + xcd * 8192); wc[28] = ldf(ws, lo, wc_off + 28 * 1024, 0); }
          if constexpr (i == 15 || i == 17) stc(sent, ws, lo + OFF_ACT + ((4 * tC + i / 2 - 5) * 112 + 8 * cC + w) * 1024);
          if constexpr (i >= 28 - NWD0) LD_WD(i - (28 - NWD0));
          if constexpr (i == 22) *(unsigned*)(sm + L_MISC + (mm == SENT ? 12 : 16)) = 1u;
          if constexpr (i == 23) { SB(); asm volatile("s_waitcnt lgkmcnt(2)\n\ts_barrier" ::: "memory"); SB(); stats_read(); fa = *(const unsigned*)(sm + L_MISC + 12); }
          if constexpr (i == 25) stats_math(0);
          if constexpr (i == 26) stats_math(1);
          SB();
        } else {
          if constexpr (NWD0 + i < 28) LD_WD(NWD0 + i);
          if constexpr (i == J2AT) {
            SB(); asm volatile("s_waitcnt vmcnt(%0)\n\ts_barrier" :: "i"(J2AT - 3) : "memory"); SB();
            store_if(w, tag, srd, l == 0 ? 0 : OOB, OFF_FA + (tC * 16 + cC) * 128);
          }
          if constexpr (i == 20) res = ldf(ws, (l & 7) * 512 + (l >> 3) * 16, OFF_HF + (ftt * 28 + 4 * T) * 1024 + (w & 3) * 128, 0);
          if constexpr (i == FAAT - 5) fa = __builtin_amdgcn_raw_buffer_load_b32(ws, l < 14 ? l * 128 : 0, OFF_FA + tB * 2048, SC1);
          if constexpr (i == FAAT) {
            if (__builtin_amdgcn_ballot_w64(fa != tag) != 0) DBG(10);
            wait_words(fa, tag, srd, l < 14 ? l * 128 : 0, OFF_FA + tB * 2048);
          }
          if constexpr (i >= FAAT) LD_AV(i - FAAT, a0off);
          SB();
        }
      });
      if constexpr (ps == 0) {
        if (__builtin_amdgcn_readfirstlane(fa) != 0u) {
          DBG(7);
          BAR();
          poll71(hv, ws, l16, hoff + 56 * 1024, vs, hso, 8 DBGP);
#pragma unroll
          for (int f = 0; f < 8; f++) lds_wr(sm, f < 7 ? h1w + f * 1024 : hsw, hv[f]);
          BAR();
          stats_read(); stats_math(0); stats_math(1);
        }
      }
      STAMP(17 + 2 * ps);
    });
    {
      int lo = fresh(l16);
      v4u av2[14];
      const rsrc_t os2 = mk(p.out, S * D * 2);
      const int oo = (ftt * 32 + 8 * (w & 3) + (l >> 3)) * 896 + 128 * T + (l & 7) * 16;
      int rw[2];
#pragma unroll
      for (int e = 0; e < 2; e++) rw[e] = w * 8192 + hh * 512 + (r & 16) * 16 + ((r + 8 * e + 4 * hh) & 15) * 16;
      const int tk = 8 * (w & 3) + (l >> 3), rr = (l & 7) * 512 + (tk & 16) * 16 + ((tk + 4 * (l & 3)) & 15) * 16;
#define LD_AV2(i) av2[i] = ldf(ws, lo, a0off + 2 * 112 * 1024 + wrap(((i) & ~1) + rd, 14) * 1024, (i) & 1)
      {
        f16v acc0 = {}, acc1 = {};
        sfor<0, 14>([&](auto II) {
          constexpr int i = decltype(II)::v;
          if constexpr (i % 4 == 0) __builtin_amdgcn_s_setprio(3 - i / 4);
          if constexpr (28 - FAAT + 2 * i < 14) LD_AV(28 - FAAT + 2 * i, a0off);
          if constexpr (28 - FAAT + 2 * i + 1 < 14) LD_AV(28 - FAAT + 2 * i + 1, a0off);
          acc0 = mfma32(wd[2 * i], av[i], acc0);
          if constexpr (i < 4) { epi(IC<1>{}, IC<4 * i>{}); epi(IC<1>{}, IC<4 * i + 1>{}); }
          SB();
          acc1 = mfma32(wd[2 * i + 1], av[i], acc1);
          if constexpr (i < 4) { epi(IC<1>{}, IC<4 * i + 2>{}); epi(IC<1>{}, IC<4 * i + 3>{}); }
          SB();
          if constexpr (i == 3) { STAMP(20); asm volatile("s_barrier" ::: "memory"); SB(); }
          if constexpr (i >= AV2AT) { LD_AV2(i - AV2AT); SB(); }
        });
        STAMP(21);
        sfor<0, 8>([&](auto QQ) {
          constexpr int q = decltype(QQ)::v;
          if constexpr (14 - AV2AT + q < 14) { LD_AV2(14 - AV2AT + q); }
          const f16v& a_ = q < 4 ? acc0 : acc1;
          lds_wr(sm, L_H0 + rw[q & 1] + q * 1024, f4u(a_[4 * (q & 3)], a_[4 * (q & 3) + 1], a_[4 * (q & 3) + 2], a_[4 * (q & 3) + 3]));
          SB();
        });
#pragma unroll
        for (int i = 22 - AV2AT; i < 14; i++) LD_AV2(i);
        SB();
      }
      {
        f16v acc0 = {}, acc1 = {};
        sfor<0, 14>([&](auto II) {
          constexpr int i = decltype(II)::v;
          if constexpr (i % 4 == 0) __builtin_amdgcn_s_setprio(3 - i / 4);
          acc0 = mfma32(wd[2 * i], av2[i], acc0); acc1 = mfma32(wd[2 * i + 1], av2[i], acc1);
          SB();
        });
        if (__builtin_amdgcn_ballot_w64(acc0[0] != acc0[0]) != 0) {
          DBG(11);
#pragma nounroll
          for (int spin = 0; spin < SPIN_CAP; spin++) {
            MEMBAR();
            unsigned mm = 0;
            lo = fresh(l16);
#pragma unroll
            for (int i = 0; i < 14; i++) LD_AV2(i);
#pragma unroll
            for (int i = 0; i < 14; i++) mm = mx4(mm, av2[i]);
            if (!bad(mm)) break;
          }
          acc0 = f16v{}; acc1 = f16v{};
#pragma unroll
          for (int i = 0; i < 14; i++) { acc0 = mfma32(wd[2 * i], av2[i], acc0); acc1 = mfma32(wd[2 * i + 1], av2[i], acc1); }
        }
        STAMP(22);
        const int r0 = fresh(L_H1 + rw[0]), r1 = fresh(L_H1 + rw[1]);
#pragma unroll
        for (int q = 0; q < 4; q++) {
          lds_wr(sm, ((q & 1) ? r1 : r0) + q * 1024, f4u(acc0[4 * q], acc0[4 * q + 1], acc0[4 * q + 2], acc0[4 * q + 3]));
          lds_wr(sm, ((q & 1) ? r1 : r0) + (4 + q) * 1024, f4u(acc1[4 * q], acc1[4 * q + 1], acc1[4 * q + 2], acc1[4 * q + 3]));
        }
        BAR();
        PIN(res);
        __builtin_amdgcn_raw_buffer_store_b128(red_final<16>(sm, fresh((w < 4 ? L_H0 : L_H1) + rr), res), os2, oo, 0, OAUX);
      }
    }
  }
  asm volatile(".Lfb_end:");
  STAMP(23);
}

extern "C" void kernel_launch(void* const* d_in, const int* in_sizes, int n_in, void* d_out, int out_size, void* d_ws, size_t ws_size, hipStream_t stream) {
  Ptrs p;
  p.x = d_in[0]; p.wqkv = d_in[1]; p.wo = d_in[2]; p.wg = d_in[4]; p.wu = d_in[5]; p.wd = d_in[6]; p.out = (char*)d_out; p.ws = (char*)d_ws;
  fused_block<<<256, 512, 0, stream>>>(p);
}
```

```cpp
#include <hip/hip_runtime.h>
#include <stdint.h>

constexpr int S = 2048, D = 448, F = 1792;
typedef unsigned v4u __attribute__((ext_vector_type(4)));
typedef unsigned v2u __attribute__((ext_vector_type(2)));
typedef float f4 __attribute__((ext_vector_type(4)));
typedef float f2 __attribute__((ext_vector_type(2)));
typedef float f16v __attribute__((ext_vector_type(16)));
typedef __bf16 bf2 __attribute__((ext_vector_type(2)));
typedef __bf16 bf8 __attribute__((ext_vector_type(8)));
typedef int srd_t __attribute__((ext_vector_type(4)));
typedef __amdgpu_buffer_rsrc_t rsrc_t;
#define DI __device__ __forceinline__
#define XNT ""
#define WNT ""
#define SB() __builtin_amdgcn_sched_barrier(0)
#define MEMBAR() asm volatile("" ::: "memory")
#define BAR() do { SB(); asm volatile("s_waitcnt lgkmcnt(0)\n\ts_barrier" ::: "memory"); SB(); } while (0)

constexpr int OFF_EPOCH = 0, OFF_FA = 32768;
constexpr int OFF_FS = 65536, OFF_CS = OFF_FS + 65536  , OFF_FD = OFF_CS + 65536;
constexpr int OFF_WC = 262144;
constexpr int OFF_WD = OFF_WC + 112 * 29 * 1024;
constexpr int OFF_PART = OFF_WD + 1568 * 1024;
constexpr int OFF_KVF = OFF_PART + 2048 * 1024;
constexpr int OFF_PQ = OFF_KVF + 32 * 1024;
constexpr int OFF_HF = OFF_PQ + 2048 * 1024;
constexpr int OFF_HS = OFF_HF + 1792 * 1024;
constexpr int OFF_ACT = OFF_HS + 128 * 1024;
constexpr int WS_BYTES = OFF_ACT + 7168 * 1024;
constexpr int OOB = 0x40000000;
constexpr int SC1 = 16;
constexpr unsigned SENT = 0xFFFFFFFFu;
constexpr int SPIN_CAP = 1 << 16;

#define STAMP(i)
#define STAMPX(set, i)
#define DBG(i)
#define DBGA
#define DBGP

DI rsrc_t mk(const void* p, int bytes) { return __builtin_amdgcn_make_buffer_rsrc((void*)p, 0, bytes, 0x00020000); }
DI v4u ldf(rsrc_t r, int voff, int base, int i) { return __builtin_amdgcn_raw_buffer_load_b128(r, voff + (i & 3) * 1024, base + (i >> 2) * 4096, SC1); }
DI void stc(v4u v, rsrc_t r, int off) { __builtin_amdgcn_raw_buffer_store_b128(v, r, off, 0, SC1); }
DI v4u canon(v4u v) { v4u o; for (int i = 0; i < 4; i++) o[i] = min(v[i], SENT - 1u); return o; }
DI f16v mfma32(v4u a, v4u b, f16v c) { return __builtin_amdgcn_mfma_f32_32x32x16_bf16(__builtin_bit_cast(bf8, a), __builtin_bit_cast(bf8, b), c, 0, 0, 0); }
DI f4 mfma16(v4u a, v4u b, f4 c) { return __builtin_amdgcn_mfma_f32_16x16x32_bf16(__builtin_bit_cast(bf8, a), __builtin_bit_cast(bf8, b), c, 0, 0, 0); }
DI unsigned pk2(float lo, float hi) { f2 v = {lo, hi}; return __builtin_bit_cast(unsigned, __builtin_convertvector(v, bf2)); }
DI float dot2(unsigned a, unsigned b, float c) { return __builtin_amdgcn_fdot2_f32_bf16(__builtin_bit_cast(bf2, a), __builtin_bit_cast(bf2, b), c, false); }
DI float bflo(unsigned u) { return __uint_as_float(u << 16); }
DI float bfhi(unsigned u) { return __uint_as_float(u & 0xffff0000u); }
DI float phi(float t) { return (fmaxf(t, 0.f) + 1e-6f) + __builtin_amdgcn_fmed3f(__builtin_amdgcn_exp2f(t * 1.44269504089f), 0.f, 1.f); }
constexpr unsigned ONES = 0x3f803f80u;
DI v4u lds_rd(const char* sm, int off) { return *(const v4u*)(sm + off); }
DI void lds_wr(char* sm, int off, v4u v) { *(v4u*)(sm + off) = v; }
DI v4u f4u(float a, float b, float c, float d) { v4u r = {__float_as_uint(a), __float_as_uint(b), __float_as_uint(c), __float_as_uint(d)}; return r; }
DI unsigned mx4(unsigned m, v4u v) { return max(max(max(m, v[0]), v[1]), max(v[2], v[3])); }
DI bool bad(unsigned m) { return __builtin_amdgcn_ballot_w64(m == SENT) != 0; }
DI float sum16(float v) { auto s = __builtin_amdgcn_permlane16_swap(__float_as_uint(v), __float_as_uint(v), false, false); return __uint_as_float(s[0]) + __uint_as_float(s[1]); }
DI float sum32(float v) { auto s = __builtin_amdgcn_permlane32_swap(__float_as_uint(v), __float_as_uint(v), false, false); return __uint_as_float(s[0]) + __uint_as_float(s[1]); }
template <int CTRL> DI float dpp_add(float v) { return v + __builtin_bit_cast(float, __builtin_amdgcn_update_dpp(0, __builtin_bit_cast(int, v), CTRL, 0xf, 0xf, true)); }
DI float wave_sum(float v) { v = dpp_add<0xB1>(v); v = dpp_add<0x4E>(v); v = dpp_add<0x141>(v); v = dpp_add<0x140>(v); return sum32(sum16(v)); }
DI int fresh(int v) { asm volatile("" : "+v"(v)); return v; }
#define PIN(x) asm volatile("" : "+v"(x))
DI unsigned swiglu2(float g0, float g1, float u0, float u1, float c, float v) {
  float t0, t1, p0, p1; unsigned r;
  asm volatile("v_mul_f32 %0, %5, %9\n\tv_mul_f32 %1, %6, %9\n\tv_exp_f32 %0, %0\n\tv_exp_f32 %1, %1\n\t"
               "v_mul_f32 %2, %5, %7\n\tv_mul_f32 %3, %6, %8\n\tv_fma_f32 %0, %0, %10, %10\n\tv_fma_f32 %1, %1, %10, %10\n\t"
               "v_rcp_f32 %0, %0\n\tv_rcp_f32 %1, %1\n\tv_mul_f32 %2, %2, %0\n\tv_mul_f32 %3, %3, %1\n\t"
               "v_cvt_pk_bf16_f32 %4, %2, %3\n\tv_min_u32 %4, -2, %4"
               : "=&v"(t0), "=&v"(t1), "=&v"(p0), "=&v"(p1), "=&v"(r) : "v"(g0), "v"(g1), "v"(u0), "v"(u1), "v"(c), "v"(v));
  return r;
}
DI int sfresh(int v) { asm volatile("" : "+s"(v)); return v; }
DI int wrap(int v, int n) { return v >= n ? v - n : v; }
template <int I> struct IC { static constexpr int v = I; };
template <int I, int N, class Fn> DI void sfor(Fn&& f) { if constexpr (I < N) { f(IC<I>{}); sfor<I + 1, N>(f); } }

struct Ptrs { const void *x, *wqkv, *wo, *wg, *wu, *wd; char* out; char* ws; };

DI srd_t mks(const void* p, int bytes) { return srd_t{(int)(size_t)p, (int)((size_t)p >> 32) & 0xffff, bytes, 0x00020000}; }
DI v4u tag4(f4 v, unsigned tag) { v4u o; for (int i = 0; i < 4; i++) o[i] = __builtin_amdgcn_perm(__float_as_uint(v[i]), tag, 0x07060500u); return o; }
DI unsigned tagx(unsigned m, v4u v, unsigned tag) { return m | ((v[0] ^ tag) | (v[1] ^ tag)) | ((v[2] ^ tag) | (v[3] ^ tag)); }
DI unsigned lo2(unsigned a, unsigned b) { return __builtin_amdgcn_perm(b, a, 0x05040100u); }
DI unsigned hi2(unsigned a, unsigned b) { return __builtin_amdgcn_perm(b, a, 0x07060302u); }
DI void wait_flags(rsrc_t ws, int off, unsigned long long lines, int l, unsigned tag, int id DBGA) {
  const int vo = ((lines >> l) & 1) ? l * 128 : OOB;
#pragma nounroll
  for (int spin = 0; spin < SPIN_CAP; spin++) {
    MEMBAR();
    const v4u f = ldf(ws, vo, off, 0);
    if ((__builtin_amdgcn_ballot_w64(((f[0] ^ tag) | (f[1] ^ tag) | (f[2] ^ tag) | (f[3] ^ tag)) != 0u) & lines) == 0) break;
    DBG(id);
    __builtin_amdgcn_s_sleep(2);
  }
}
DI void set_flag(rsrc_t ws, int off, int b, bool on, int l, unsigned tag) {
  __builtin_amdgcn_raw_buffer_store_b32(tag, ws, (on && l < 8) ? l * 8192 : OOB, off + (b >> 2) * 128 + (b & 3) * 4, SC1);
}
DI void wait_words(unsigned v, unsigned tag, srd_t srd, int voff, int soff) {
  int cnt;
  asm volatile("s_mov_b32 %1, 0x10000\n"
    "1:\n\tv_cmp_ne_u32 vcc, %0, %2\n\ts_cbranch_vccz 2f\n\t"
    "buffer_load_dword %0, %3, %4, %5 offen sc1\n\ts_waitcnt vmcnt(0)\n\t"
    "s_sub_u32 %1, %1, 1\n\ts_cbranch_scc0 1b\n"
    "2:" : "+v"(v), "=&s"(cnt) : "v"(tag), "v"(voff), "s"(srd), "s"(soff) : "vcc", "scc", "memory");
}
template <int N> DI void poll_frags(v4u (&v)[N], rsrc_t ws, int voff, int base, int id DBGA) {
#pragma nounroll
  for (int spin = 0; spin < SPIN_CAP; spin++) {
    MEMBAR();
    const int lo = fresh(voff);
    unsigned m = 0;
#pragma unroll
    for (int i = 0; i < N; i++) v[i] = ldf(ws, lo, base, i);
#pragma unroll
    for (int i = 0; i < N; i++) m = mx4(m, v[i]);
    if (!bad(m)) break;
    DBG(id);
  }
}
DI void poll71(v4u (&v)[8], rsrc_t ws, int voff, int s0, int v1, int s1, int id DBGA) {
#pragma nounroll
  for (int spin = 0; spin < SPIN_CAP; spin++) {
    MEMBAR();
    const int lo = fresh(voff);
    unsigned m = 0;
#pragma unroll
    for (int i = 0; i < 7; i++) v[i] = ldf(ws, lo, s0, i);
    v[7] = ldf(ws, v1, s1, 0);
#pragma unroll
    for (int i = 0; i < 8; i++) m = mx4(m, v[i]);
    if (!bad(m)) break;
    DBG(id);
  }
}
DI void store_if(int c, unsigned v, srd_t srd, int voff, int soff) {
  asm volatile("s_cmp_lg_u32 %0, 0\n\ts_cbranch_scc1 1f\n\tbuffer_store_dword %1, %2, %3, %4 offen sc1\n1:" :: "s"(c), "v"(v), "v"(voff), "s"(srd), "s"(soff) : "scc", "memory");
}
template <class Fn> DI void poll8(v4u (&v)[8], rsrc_t ws, int voff, int s0, int s1, int id DBGA, Fn&& hook) {
  {
    const int lo = fresh(voff);
    unsigned m = 0;
#pragma unroll
    for (int i = 0; i < 8; i++) v[i] = ldf(ws, lo, i < 4 ? s0 : s1, i & 3);
    SB(); hook(); SB();
#pragma unroll
    for (int i = 0; i < 8; i++) m = mx4(m, v[i]);
    if (!bad(m)) return;
    DBG(id);
  }
#pragma nounroll
  for (int spin = 0; spin < SPIN_CAP; spin++) {
    MEMBAR();
    const int lo = fresh(voff);
    unsigned m = 0;
#pragma unroll
    for (int i = 0; i < 8; i++) v[i] = ldf(ws, lo, i < 4 ? s0 : s1, i & 3);
#pragma unroll
    for (int i = 0; i < 8; i++) m = mx4(m, v[i]);
    if (!bad(m)) break;
    DBG(id);
  }
}
typedef short s4v __attribute__((ext_vector_type(4)));
#define LAS __attribute__((address_space(3)))
DI v4u tr_frag(const char* sm, int off) {
  const v2u a = __builtin_bit_cast(v2u, __builtin_amdgcn_ds_read_tr16_b64_v4i16((LAS s4v*)((LAS char*)sm + off)));
  const v2u b = __builtin_bit_cast(v2u, __builtin_amdgcn_ds_read_tr16_b64_v4i16((LAS s4v*)((LAS char*)sm + off + 448)));
  return v4u{a[0], a[1], b[0], b[1]};
}
DI void dma_kv(int voff, srd_t srd, int soff, int lds) {
  unsigned keep;
  asm volatile("s_mov_b32 %0, m0\n\ts_mov_b32 exec_hi, 0x00ffffff\n\ts_mov_b32 m0, %4\n\ts_nop 0\n\t"
               "buffer_load_dwordx4 %1, %2, %3 offen offset:896" WNT " lds\n\tbuffer_load_dwordx4 %1, %2, %3 offen offset:1792" WNT " lds\n\t"
               "s_mov_b32 m0, %0\n\ts_mov_b32 exec_hi, -1" : "=&s"(keep) : "v"(voff), "s"(srd), "s"(soff), "s"(lds - 896) : "memory");
}
DI void dma_q(int voff, srd_t srd, int soff, int lds) {
  unsigned keep;
  asm volatile("s_mov_b32 %0, m0\n\ts_mov_b32 exec_hi, 0x00ffffff\n\ts_mov_b32 m0, %4\n\ts_nop 0\n\t"
               "buffer_load_dwordx4 %1, %2, %3 offen" WNT " lds\n\t"
               "s_mov_b32 m0, %0\n\ts_mov_b32 exec_hi, -1" : "=&s"(keep) : "v"(voff), "s"(srd), "s"(soff), "s"(lds) : "memory");
}
DI void lds_arrive(char* sm, int off, int l) { if (l == 0) __hip_atomic_fetch_add((unsigned*)(sm + off), 1u, __ATOMIC_RELAXED, __HIP_MEMORY_SCOPE_WORKGROUP); }
DI void lds_arrive_nb(int addr_lane0, int addr_other, int l) { asm volatile("ds_add_u32 %0, %1" :: "v"(l == 0 ? addr_lane0 : addr_other + l * 4), "v"(1) : "memory"); }
DI void lds_wait(int addr, unsigned n) {
  unsigned t;
  asm volatile("1:\n\tds_read_b32 %0, %1\n\ts_waitcnt lgkmcnt(0)\n\tv_cmp_gt_u32 vcc, %2, %0\n\ts_cbranch_vccnz 1b" : "=&v"(t) : "v"(addr), "s"(n) : "vcc", "memory");
}
DI void dma_full_nt(int voff, srd_t srd, int soff, int lds) {
  unsigned keep;
  asm volatile("s_mov_b32 %0, m0\n\ts_mov_b32 m0, %4\n\ts_nop 0\n\tbuffer_load_dwordx4 %1, %2, %3 offen nt lds\n\ts_mov_b32 m0, %0" : "=&s"(keep) : "v"(voff), "s"(srd), "s"(soff), "s"(lds) : "memory");
}
DI void dma_full(int voff, srd_t srd, int soff, int lds) {
  unsigned keep;
  asm volatile("s_mov_b32 %0, m0\n\ts_mov_b32 m0, %4\n\ts_nop 0\n\tbuffer_load_dwordx4 %1, %2, %3 offen lds\n\ts_mov_b32 m0, %0" : "=&s"(keep) : "v"(voff), "s"(srd), "s"(soff), "s"(lds) : "memory");
}
DI v2u tr_half(const char* sm, int off) { return __builtin_bit_cast(v2u, __builtin_amdgcn_ds_read_tr16_b64_v4i16((LAS s4v*)((LAS char*)sm + off))); }
template <int G> DI v4u red_final(const char* sm, int off, v4u res) {
  f4 e = {bflo(res[0]), bflo(res[1]), bflo(res[2]), bflo(res[3])}, o = {bfhi(res[0]), bfhi(res[1]), bfhi(res[2]), bfhi(res[3])};
#pragma unroll
  for (int g = 0; g < 16; g += G) {
    v4u t[G];
#pragma unroll
    for (int i = 0; i < G; i++) t[i] = lds_rd(sm, off + (g + i) * 4096);
    SB();
#pragma unroll
    for (int w = 0; w < G / 2; w++) { e += __builtin_bit_cast(f4, t[2 * w]); o += __builtin_bit_cast(f4, t[2 * w + 1]); }
    SB();
  }
  v4u out;
#pragma unroll
  for (int i = 0; i < 4; i++) out[i] = pk2(e[i], o[i]);
  return out;
}

constexpr int L_X = 0;
constexpr int L_KV = 57344;
constexpr int L_KVIN = 114688;
constexpr int L_WO = 57344;
constexpr int L_OB0 = 0, L_OB1 = 114688;
constexpr int L_H0 = 0;
constexpr int L_H1 = 65536;
constexpr int L_HST = 131328;
constexpr int L_XN = 149504;
constexpr int L_MISC = 157696;
constexpr int L_DUMMY = 157824;
constexpr int L_CS = 158080;
constexpr int L_BYTES = 162176;

#define ABL 0
#define DLY_X 20
#define DLY_AV 10
#define AV2AT 14
#define WARM 0
#define ROTD 0
#define TFIRST 0
#define UNT " nt"
#define DLY_KQ 4
#define J2AT 11
#define JAT 22
#define FAAT 25
#define OAUX 16
#define NB0 2
#define NB1 2
#define NB2 3
#define NB3 2
#define NPFB (NPF1 + 4 * NB0 + NB1 + NB2 + NB3)
#define ROTC 0
#define DLY_PQ 0
#define DLY_KF 4
#define DLY_F1 8
#define DLY_KV 24
#define NPF0 6
#define NPF1 9
#define NPF2 9
#define NPF3 15
#define NWD0 8

__global__ __launch_bounds__(512, 2) void fused_block(Ptrs p) {
  __shared__ __attribute__((aligned(16))) char sm[L_BYTES];
  STAMP(0);
  const rsrc_t ws = mk(p.ws, WS_BYTES);
  const srd_t srd = mks(p.ws, WS_BYTES);
  const int b = blockIdx.x, xcd = b & 7, idx = b >> 3;
  const int w = __builtin_amdgcn_readfirstlane(threadIdx.x >> 6), l = threadIdx.x & 63, l16 = l * 16;
  unsigned ep, ut[8];
  {
    unsigned long long pc; int len;
    asm volatile("s_getpc_b64 %0\n.Lfb_pc:\n\ts_mov_b32 %1, .Lfb_end-.Lfb_pc" : "=s"(pc), "=s"(len));
    const void* eb = w == 0 ? (const void*)p.ws : (w == 1 ? p.x : (w == 2 ? p.wqkv : (w == 3 ? p.wo : (const void*)pc)));
    const int en = w == 0 ? 64 : (w == 4 ? len : (!TFIRST || w > 4 ? 0 : (w == 1 ? S * D * 2 : (w == 2 ? D * 3 * D * 2 : D * D * 2))));
    const int nl = w == 1 ? 56 : (w == 2 ? 37 : (w == 3 ? 13 : 32)), e0 = (w == 4 ? (idx & 7) : b) * nl;
    asm volatile("buffer_load_dword %0, %1, %2, 0 offen sc1" : "=v"(ep) : "v"(w == 0 ? 0 : (l < nl ? (e0 + l) * 128 : OOB)), "s"(mks(eb, en)) : "memory");
  }
  const int uks = 7 * ((b >> 1) & 3) + (w ? w - 1 : 0);
  asm volatile("s_barrier" ::: "memory");
  {
    const srd_t us = mks((b & 1) ? p.wu : p.wg, D * F * 2);
    const int uv = (b < 224 && w) ? (l >> 5) * 28672 + (l & 31) * 4 : OOB, uo = uks * 57344 + idx * 128;
#pragma unroll
    for (int j = 0; j < 8; j++) asm volatile("buffer_load_dword %0, %1, %2, %3 offen" UNT : "=v"(ut[j]) : "v"(uv), "s"(us), "s"(uo + j * 3584) : "memory");
  }
  const int t64 = 8 * (xcd & 3) + (idx & 7), hp = xcd < 4 ? 3 - (idx >> 3) : 4 + (idx >> 3);
  const int rot = wrap((idx & 7) + 2 * (idx >> 3), 7) % 7;
  v4u xg[7];
  {
    const srd_t xs = mks(p.x, S * D * 2);
    const int xv = (l >> 3) * 896 + (l & 7) * 16, xso = (t64 * 64 + w * 8) * 896;
#pragma unroll
    for (int i = 0; i < 7; i++) asm volatile("buffer_load_dwordx4 %0, %1, %2, %3 offen" XNT : "=v"(xg[i]) : "v"(xv), "s"(xs), "s"(xso + wrap(i + rot, 7) * 128) : "memory");
  }
  asm volatile("s_barrier" ::: "memory");
  const srd_t wsrd = mks(p.wqkv, D * 3 * D * 2);
  const int l7 = (l * 37) >> 8, wv = l7 * 2688 + (l - 7 * l7) * 16 + hp * 112;
  const int lbase = (int)(size_t)(LAS char*)sm;
#define DMA_KV(c) dma_kv(wv, wsrd, (wrap((c) + rot, 7) * 8 + w) * 21504, lbase + L_KV + ((c) * 8 + w) * 1792)
#define DMA_Q(c) dma_q(wv, wsrd, (wrap((c) + rot, 7) * 8 + w) * 21504, lbase + L_X + ((c) * 8 + w) * 896)
  const int th2 = idx >= 14, tB = 2 * xcd + th2, m = idx - 14 * th2, T = m >> 1, a = m & 1;
  const bool ffn = idx < 28;
  const int r = l & 31, c16 = l & 15, g4 = l >> 4, hh = l >> 5;
  const v4u sent = {SENT, SENT, SENT, SENT};
  const int tt = w & 1, hd = (w >> 1) & 1;
  const int pq_off = OFF_PQ + ((2 * t64 + tt) * 16 + 2 * hp + hd) * 2048 + l16;
  const int part_off = OFF_PART + ((2 * hp + hd) * 32 + t64) * 4096 + (w & 1) * 2048 + l16;
  DMA_KV(0);
  SB();
  const int ai = idx, tC = 4 * (xcd & 3) + (ai & 3), cC = 7 * (xcd >> 2) + (ai >> 2);
  const int jcs = b - 224;
  *(v4u*)(sm + L_MISC + (threadIdx.x == 0 ? 0 : (threadIdx.x == 1 ? 32 : 16))) = v4u{0u, 0u, 0u, 0u};
  SB();
  asm volatile("s_waitcnt vmcnt(2)" : "+v"(ep), "+v"(xg[0]), "+v"(xg[1]), "+v"(xg[2]), "+v"(xg[3]), "+v"(xg[4]), "+v"(xg[5]), "+v"(xg[6]));
  STAMP(1);
  DMA_KV(1);
  {
    float s = 0.f, ss = 0.f;
#pragma unroll
    for (int i = 0; i < 7; i++)
#pragma unroll
      for (int j = 0; j < 4; j++) { s = dot2(xg[i][j], ONES, s); ss = dot2(xg[i][j], xg[i][j], ss); }
    SB(); DMA_KV(2); SB();
    s = dpp_add<0x141>(dpp_add<0x4E>(dpp_add<0xB1>(s))); ss = dpp_add<0x141>(dpp_add<0x4E>(dpp_add<0xB1>(ss)));
    const float mean = s * (1.f / D), var = fmaxf(ss * (1.f / D) - mean * mean, 0.f);
    const float ra = __builtin_amdgcn_rsqf(var + 1e-5f), rb = -mean * ra;
    const int o = L_X + w * 7168 + (l >> 3) * 128 + (((l & 7) ^ (4 * (w & 1) + (l >> 4))) * 16);
#pragma unroll
    for (int i = 0; i < 7; i++) {
      float f[8];
#pragma unroll
      for (int j = 0; j < 4; j++) { f[2 * j] = bflo(xg[i][j]); f[2 * j + 1] = bfhi(xg[i][j]); }
#pragma unroll
      for (int j = 0; j < 8; j++) { f[j] = __builtin_fmaf(f[j], ra, rb); asm volatile("" : "+v"(f[j])); }
      const v4u xn = {pk2(f[0], f[1]), pk2(f[2], f[3]), pk2(f[4], f[5]), pk2(f[6], f[7])};
      lds_wr(sm, o + i * 1024, xn);
      if (i == 3) { SB(); DMA_KV(3); SB(); }
    }
  }
  *(unsigned*)(sm + L_MISC + (threadIdx.x == 0 ? 0 : 16)) = ep + 1u;
  SB();
  DMA_KV(4);
  BAR();
  STAMP(2);
  const unsigned tag = __builtin_amdgcn_readfirstlane(*(const unsigned*)(sm + L_MISC));
  SB();
  v4u xf[28];
  {
    const int row = 32 * tt + r, xrb = L_X + (row >> 3) * 7168 + (row & 7) * 128, sw = (row >> 1) & 7;
    int xa[4];
#pragma unroll
    for (int i = 0; i < 4; i++) xa[i] = xrb + (((2 * i + hh) ^ sw) * 16);
#pragma unroll
    for (int i = 0; i < 28; i++) xf[i] = lds_rd(sm, xa[i & 3] + (i >> 2) * 1024);
  }
  const int trl = (c16 >> 2) * 112 + hd * 56 + min(4 * (g4 & 1) + (c16 & 3), 6) * 8;
  f16v acc = {};
  const int ba = fresh(L_KV + (w >> 2) * 896 + hh * 1792 + trl), bb2 = fresh(L_KV + (w >> 2) * 896 + hh * 1792 + trl + 57344);
#define RD_KV(i) tr_frag(sm, (i) < 16 ? ba + (i) * 3584 : bb2 + ((i) - 16) * 3584)
  v4u bb[4];
  {
    SB();
    {
      asm volatile("s_waitcnt vmcnt(8)" : "+v"(ut[0]), "+v"(ut[1]), "+v"(ut[2]), "+v"(ut[3]), "+v"(ut[4]), "+v"(ut[5]), "+v"(ut[6]), "+v"(ut[7]));
      const int d0 = w ? (ffn ? OFF_WC + (116 * idx + uks) * 1024 + (g4 & 1) * 59392 + hh * 512 + (b & 1) * 256 + c16 * 16 : OOB) : (ffn ? OOB : l16 + OFF_KVF + jcs * 1024);
      const int d1 = w ? d0 + 29696 : ((ffn && l < 32) ? l16 + OFF_HS + ((tB * 8 + T) * 2 + a) * 512 : OOB);
      const unsigned f_ = w ? 0u : SENT;
      const v4u ue = {lo2(ut[0], ut[1]), lo2(ut[2], ut[3]), lo2(ut[4], ut[5]), lo2(ut[6], ut[7])}, uo_ = {hi2(ut[0], ut[1]), hi2(ut[2], ut[3]), hi2(ut[4], ut[5]), hi2(ut[6], ut[7])};
      stc(ue | f_, ws, d0); stc(uo_ | f_, ws, d1);
      f2 cs = {0.f, 0.f};
#pragma unroll
      for (int j = 0; j < 4; j++) { cs[0] = dot2(ue[j], ONES, cs[0]); cs[1] = dot2(uo_[j], ONES, cs[1]); }
      *(f2*)(sm + L_CS + threadIdx.x * 8) = cs;
      SB(); DMA_KV(5); SB();
      stc(sent, ws, pq_off + (w >> 2) * 1024);
      stc(sent, ws, ffn ? l16 + OFF_HF + ((4 * tB + a + 2 * (w >> 2)) * 28 + 4 * T + (w & 3)) * 1024 : OOB);
      SB(); DMA_KV(6);
    }
    SB(); asm volatile("s_waitcnt vmcnt(10) lgkmcnt(0)\n\ts_barrier" ::: "memory"); SB();
#pragma unroll
    for (int i = 0; i < 3; i++) bb[i] = RD_KV(i);
    sfor<0, 13>([&](auto II) {
      constexpr int i = decltype(II)::v;
      if constexpr (i % 7 == 0) __builtin_amdgcn_s_setprio(3 - i / 7);
      if constexpr (i == 4 || i == 8 || i == 12) DMA_Q(i / 4 - 1);
      bb[(i + 3) & 3] = RD_KV(i + 3);
      acc = mfma32(xf[i], bb[i & 3], acc);
      SB();
    });
    asm volatile("s_waitcnt vmcnt(3) lgkmcnt(0)\n\ts_barrier" ::: "memory"); SB();
    STAMP(3);
  }
  v4u wc[29];
  const int wc_off = OFF_WC + (8 * cC + w) * 29 * 1024;
#define LD_WC(i) wc[i] = ldf(ws, lo, wc_off, i)
  const srd_t os = mks(ffn ? p.wo : p.wd, ffn ? D * D * 2 : F * D * 2);
  const int ov = ffn ? (l >> 3) * 896 + (((l & 7) ^ (2 * ((l >> 4) & 3))) * 16) + 128 * T : (jcs < 28 ? l16 + jcs * 57344 : OOB), og = ffn ? 14 * (2 * th2 + a) : 0, ost = ffn ? 7168 : 1024;
#define DMA_WO(g) do { const int gg_ = wrap((g) + og, 56); dma_full(ov, os, gg_ * ost, lbase + L_WO + gg_ * 1024); } while (0)
  if (w < 4) {
    __builtin_amdgcn_s_setprio(3);
    sfor<13, 28>([&](auto II) {
      constexpr int i = decltype(II)::v;
      if constexpr (i == 21) { SB(); lds_arrive_nb(lbase + L_MISC + 32, lbase + L_DUMMY, l); SB(); }
      if constexpr (i + 3 < 28) bb[(i + 3) & 3] = RD_KV(i + 3);
      acc = mfma32(xf[i], bb[i & 3], acc);
      SB();
    });
    STAMP(4);
    {
      v4u fr[2];
#pragma unroll
      for (int j = 0; j < 8; j++) fr[j >> 2][j & 3] = pk2(phi(acc[2 * j]), phi(acc[2 * j + 1]));
      SB(); lds_wait(lbase + L_MISC + 32, 8u);
      lds_wr(sm, L_KVIN + w * 2048 + l16, fr[0]); lds_wr(sm, L_KVIN + w * 2048 + 1024 + l16, fr[1]);
    }
    SB(); asm volatile("s_waitcnt vmcnt(0) lgkmcnt(0)\n\ts_barrier" ::: "memory"); SB();
    STAMP(5);
    {
      v4u ka[2], vb[2][2];
      const int ko = L_KVIN + hd * 4096 + g4 * 512 + c16 * 16;
#pragma unroll
      for (int t_ = 0; t_ < 2; t_++) { ka[t_] = lds_rd(sm, ko + t_ * 2048 + (w & 1) * 256); vb[t_][0] = lds_rd(sm, ko + 8192 + t_ * 2048); vb[t_][1] = lds_rd(sm, ko + 8192 + t_ * 2048 + 256); }
      SB();
      f4 kv[2] = {};
#pragma unroll
      for (int t_ = 0; t_ < 2; t_++) { kv[0] = mfma16(ka[t_], vb[t_][0], kv[0]); kv[1] = mfma16(ka[t_], vb[t_][1], kv[1]); }
      stc(tag4(kv[0], tag), ws, part_off); stc(tag4(kv[1], tag), ws, part_off + 1024);
    }
    STAMP(6);
    if (w == 2) {
      float cs = 0.f;
#pragma unroll
      for (int i = 2; i < 16; i++) cs += *(const float*)(sm + L_CS + i * 256 + l * 4);
      __builtin_amdgcn_raw_buffer_store_b32(__builtin_amdgcn_perm(__float_as_uint(cs), tag, 0x07060500u), ws, ffn ? l * 4 : OOB, OFF_CS + (b & 7) * 7168 + idx * 256, SC1);
    }
    if (w == 3 && ffn) {
      wait_flags(ws, OFF_FS + xcd * 8192, ~0ull, l, tag, 0 DBGP);
      if (b == 0 && l == 0) __builtin_amdgcn_raw_buffer_store_b32(tag, mk(p.ws, 64), 0, OFF_EPOCH, SC1);
      lds_arrive(sm, L_MISC + 4, l);
    } else if (ffn) {
      __builtin_amdgcn_s_setprio(0);
#pragma nounroll
      for (int g = w; g < 56; g += 3) DMA_WO(g);
    }
    __builtin_amdgcn_s_setprio(0);
  } else {
    __builtin_amdgcn_s_setprio(0);
    sfor<13, 28>([&](auto II) {
      constexpr int i = decltype(II)::v;
      if constexpr (i == 13) store_if(w - 7, tag, srd, l < 8 ? l * 8192 : OOB, OFF_FS + (b >> 2) * 128 + (b & 3) * 4);
      if constexpr (i == 21) { SB(); lds_arrive_nb(lbase + L_MISC + 32, lbase + L_DUMMY, l); SB(); }
      if constexpr (i == 15 || i == 18 || i == 22 || i == 25) {
        constexpr int c = i == 15 ? 3 : (i == 18 ? 4 : (i == 22 ? 5 : 6));
        dma_q(wv, wsrd, (wrap(c + rot, 7) * 8 + w - 4) * 21504, lbase + L_X + (c * 8 + w - 4) * 896); DMA_Q(c);
      }
      if constexpr (i + 3 < 28) bb[(i + 3) & 3] = RD_KV(i + 3);
      acc = mfma32(xf[i], bb[i & 3], acc);
      SB();
    });
    STAMP(4);
    {
      const unsigned pad = r == 28 ? ONES : 0u;
      v4u fr[2];
#pragma unroll
      for (int j = 0; j < 8; j++) { const unsigned t_ = pk2(acc[2 * j], acc[2 * j + 1]); fr[j >> 2][j & 3] = r < 28 ? t_ : pad; }
      SB(); lds_wait(lbase + L_MISC + 32, 8u);
      lds_wr(sm, L_KVIN + w * 2048 + l16, fr[0]); lds_wr(sm, L_KVIN + w * 2048 + 1024 + l16, fr[1]);
    }
    BAR();
    STAMP(5);
    asm volatile("s_waitcnt vmcnt(0)" ::: "memory");
    lds_arrive(sm, L_MISC + 36, l);
    lds_wait(lbase + L_MISC + 36, 4u);
    const int qa = fresh(L_X + hh * 896 + trl);
    f16v aq = {};
#pragma unroll
    for (int i = 0; i < 3; i++) bb[i] = tr_frag(sm, qa + i * 1792);
    SB();
    sfor<0, 28>([&](auto II) {
      constexpr int i = decltype(II)::v;
      if constexpr (i + 3 < 28) bb[(i + 3) & 3] = tr_frag(sm, qa + (i + 3) * 1792);
      aq = mfma32(bb[i & 3], xf[i], aq);
      SB();
    });
    __builtin_amdgcn_s_setprio(3);
#pragma unroll
    for (int s = 0; s < 2; s++) {
      v4u pf = {pk2(phi(aq[8 * s]), phi(aq[8 * s + 1])), pk2(phi(aq[8 * s + 2]), phi(aq[8 * s + 3])), pk2(phi(aq[8 * s + 4]), phi(aq[8 * s + 5])), pk2(phi(aq[8 * s + 6]), phi(aq[8 * s + 7]))};
      if (s == 1) { pf[2] = hh ? 0u : pf[2]; pf[3] = hh ? 0u : pf[3]; }
      stc(canon(pf), ws, pq_off + s * 1024);
    }
    __builtin_amdgcn_s_setprio(0);
    STAMP(8);
  }
  SB();
  STAMP(10);
  if (!ffn) {
    {
      const int rh = jcs >> 1, rj = (jcs & 1) * 8 + w;
      v4u pv[8];
      const int so = OFF_PART + rh * 131072 + ((rj >> 3) * 2 + ((rj >> 1) & 1)) * 1024 + ((rj >> 2) & 1) * 256 + (rj & 1) * 128;
      const unsigned tb = tag & 0xffu;
      if (w < 4) __builtin_amdgcn_s_sleep(DLY_KV); else __builtin_amdgcn_s_sleep(DLY_KQ);
#pragma nounroll
      for (int spin = 0; spin < SPIN_CAP; spin++) {
        MEMBAR();
        const int vo = fresh(g4 * 4096 + (c16 >> 3) * 512 + (c16 & 7) * 16);
        unsigned mm = 0;
#pragma unroll
        for (int i = 0; i < 8; i++) pv[i] = ldf(ws, vo, so + i * 16384, 0);
#pragma unroll
        for (int i = 0; i < 8; i++) { pv[i] ^= tb; mm |= (pv[i][0] | pv[i][1]) | (pv[i][2] | pv[i][3]); }
        if (__builtin_amdgcn_ballot_w64((mm & 0xffu) != 0u) == 0) break;
        DBG(3);
      }
      f4 s = __builtin_bit_cast(f4, pv[0]);
#pragma unroll
      for (int i = 1; i < 8; i++) s += __builtin_bit_cast(f4, pv[i]);
#pragma unroll
      for (int i = 0; i < 4; i++) s[i] = sum32(sum16(s[i]));
      const v2u o = {min(pk2(s[0], s[1]), SENT - 1u), min(pk2(s[2], s[3]), SENT - 1u)};
      __builtin_amdgcn_raw_buffer_store_b64(o, ws, l < 16 ? (c16 & 7) * 16 + (c16 >> 3) * 8 : OOB, OFF_KVF + jcs * 1024 + w * 128, SC1);
      STAMP(9);
      BAR();
    }
#pragma unroll
    for (int i = 0; i < 7; i++) dma_full_nt(ov, os, (8 * i + w) * 1024, lbase + L_WO + (8 * i + w) * 1024);
    const int ao = l16 + OFF_ACT + ((4 * (jcs >> 1) + 2 + (jcs & 1)) * 112 + w) * 1024;
#pragma unroll
    for (int i = 0; i < 5; i++) stc(sent, ws, ao + i * 8192);
    asm volatile("s_waitcnt vmcnt(5)" ::: "memory");
    BAR();
    if (jcs < 28) {
      if (w < 7) {
        unsigned u_[4][8];
        const int dv = fresh(L_WO + hh * 7168 + r * 4 + 128 * w);
#pragma unroll
        for (int u = 0; u < 4; u++)
#pragma unroll
          for (int j = 0; j < 8; j++) u_[u][j] = *(const unsigned*)(sm + dv + (u & 1) * 896 + (u >> 1) * 28672 + (j & 3) * 1792 + (j >> 2) * 14336);
        SB();
#pragma unroll
        for (int u = 0; u < 4; u++) {
          const int d0 = OFF_WD + (224 * w + 4 * jcs + u) * 1024 + l16;
          stc(v4u{lo2(u_[u][0], u_[u][1]), lo2(u_[u][2], u_[u][3]), lo2(u_[u][4], u_[u][5]), lo2(u_[u][6], u_[u][7])}, ws, d0);
          stc(v4u{hi2(u_[u][0], u_[u][1]), hi2(u_[u][2], u_[u][3]), hi2(u_[u][4], u_[u][5]), hi2(u_[u][6], u_[u][7])}, ws, d0 + 114688);
        }
      }
    }
    asm volatile("s_waitcnt vmcnt(0)" ::: "memory");
    BAR();
    if (w == 0) set_flag(ws, OFF_FD, b, true, l, tag);
#pragma unroll
    for (int i = 5; i < 14; i++) stc(sent, ws, ao + i * 8192);
    asm volatile("s_waitcnt vmcnt(0)" ::: "memory");
    BAR();
    if (w == 0) set_flag(ws, OFF_FD - 4096, b, true, l, tag);
  } else {
    const int c4 = w & 3, th = w >> 2;
    v4u wo[16], pq[8], kvf[4], hv[8];
    int woa[2];
#pragma unroll
    for (int e = 0; e < 2; e++) woa[e] = L_WO + (16 * (g4 >> 1) + 4 * (g4 & 1) + (c16 >> 2)) * 128 + (((2 * c4 + ((c16 >> 1) & 1)) ^ (2 * ((((e + g4) & 1) << 1) + (c16 >> 3)))) * 16) + (c16 & 1) * 8;
    const int wo8 = g4 == 3 ? 0 : 1024;
    const int hoff = OFF_HF + (4 * tC * 28 + 7 * w) * 1024;
    v2u xr[2];
    lds_wait(lbase + L_MISC + 4, 1u);
    {
      const int lo = fresh(l16);
#pragma unroll
      for (int ps = 0; ps < 2; ps++) xr[ps] = *(const v2u*)((const char*)p.x + (size_t)((4 * tB + a + 2 * ps) * 32 + 16 * th + c16) * 896 + (64 * T + 16 * c4 + 4 * g4) * 2);
#pragma unroll
      for (int i = 0; i < NPF0; i++) LD_WC(i);
      SB();
      asm volatile("s_waitcnt vmcnt(%0)" :: "i"(NPF0 + 2) : "memory");
      lds_arrive(sm, L_MISC + 8, l);
    }
    lds_wait(lbase + L_MISC + 8, 8u);
    __builtin_amdgcn_s_setprio(2);
    poll8(pq, ws, l16, OFF_PQ + ((4 * tB + a) * 32 + 4 * w) * 1024, OFF_PQ + ((4 * tB + a + 2) * 32 + 4 * w) * 1024, 4 DBGP, [&] {
      const int lo = fresh(l16);
      __builtin_amdgcn_s_setprio(0);
#pragma unroll
      for (int i = 0; i < 16; i++) {
        const v2u x0 = tr_half(sm, woa[i & 1] + i * 3584), x1 = tr_half(sm, woa[i & 1] + wo8 + i * 3584);
        wo[i] = v4u{x0[0], x0[1], x1[0], x1[1]};
      }
      SB();
      if (DLY_KF) __builtin_amdgcn_s_sleep(DLY_KF);
      __builtin_amdgcn_s_setprio(3);
#pragma unroll
      for (int i = 0; i < 4; i++) kvf[i] = ldf(ws, lo, OFF_KVF + 4 * w * 1024, i);
      __builtin_amdgcn_s_setprio(0);
      SB();
#pragma unroll
      for (int i = NPF0; i < NPF1; i++) LD_WC(i);
    });
    STAMP(7);
    {
      unsigned mm = 0;
#pragma unroll
      for (int i = 0; i < 4; i++) mm = mx4(mm, kvf[i]);
      if (bad(mm)) { DBG(5); __builtin_amdgcn_s_setprio(3); poll_frags<4>(kvf, ws, l16, OFF_KVF + 4 * w * 1024, 5 DBGP); __builtin_amdgcn_s_setprio(0); }
    }
    STAMP(11);
    sfor<0, 2>([&](auto PS) {
      constexpr int ps = decltype(PS)::v;
      const int lo = fresh(l16);
      f16v n[2] = {};
#pragma unroll
      for (int s = 0; s < 2; s++)
#pragma unroll
        for (int hd = 0; hd < 2; hd++) n[hd] = mfma32(kvf[2 * hd + s], pq[4 * ps + 2 * hd + s], n[hd]);
      constexpr int pa = ps ? NPF1 + 2 * NB0 + NB1 + NB2 : NPF1;
#pragma unroll
      for (int i = 0; i < NB0; i++) LD_WC(pa + i);
      SB();
#pragma unroll
      for (int hd = 0; hd < 2; hd++) {
        const auto sw = __builtin_amdgcn_permlane32_swap(__float_as_uint(n[hd][12]), __float_as_uint(n[hd][12]), false, false);
        const float inv = __builtin_amdgcn_rcpf(__uint_as_float(sw[1]) + 1e-6f);
        v4u o0, o1;
#pragma unroll
        for (int i = 0; i < 4; i++) { o0[i] = pk2(n[hd][2 * i] * inv, n[hd][2 * i + 1] * inv); o1[i] = pk2(n[hd][8 + 2 * i] * inv, n[hd][9 + 2 * i] * inv); }
        o1[2] = hh ? 0u : o1[2];
        lds_wr(sm, (ps ? L_OB1 : L_OB0) + (4 * w + 2 * hd) * 1024 + lo, o0);
        lds_wr(sm, (ps ? L_OB1 : L_OB0) + (4 * w + 2 * hd + 1) * 1024 + lo, o1);
      }
      SB();
#pragma unroll
      for (int i = 0; i < NB0; i++) LD_WC(pa + NB0 + i);
      BAR();
      if (ps == 0) STAMP(12);
      v4u ob[8];
      const int oo = fresh((ps ? L_OB1 : L_OB0) + g4 * 512 + th * 256 + c16 * 16), vh = fresh(l16);
#pragma unroll
      for (int i = 0; i < 8; i++) ob[i] = lds_rd(sm, oo + i * 2048);
      f4 hacc = {bflo(xr[ps][0]), bfhi(xr[ps][0]), bflo(xr[ps][1]), bfhi(xr[ps][1])};
      SB();
#pragma unroll
      for (int i = 0; i < 16; i++) {
        hacc = mfma16(wo[i], ob[i & 7], hacc);
        if (i < 8) ob[i] = lds_rd(sm, oo + (8 + i) * 2048);
        if (ps == 0 && (i & 3) == 1 && (i >> 2) < NB1) LD_WC(pa + 2 * NB0 + (i >> 2));
        if (ps == 1 && (i & 1) && i < 14) hv[i >> 1] = ldf(ws, vh, hoff, i >> 1);
        SB();
      }
      const unsigned p0 = min(pk2(hacc[0], hacc[1]), SENT - 1u), p1 = min(pk2(hacc[2], hacc[3]), SENT - 1u);
      const auto s0 = __builtin_amdgcn_permlane16_swap(p0, p0, false, false), s1 = __builtin_amdgcn_permlane16_swap(p1, p1, false, false);
      const v4u hf = {s0[0], s1[0], s0[1], s1[1]};
      stc(hf, ws, (g4 & 1) ? OOB : OFF_HF + ((4 * tB + a + 2 * ps) * 28 + 4 * T + c4) * 1024 + (g4 >> 1) * 512 + th * 256 + c16 * 16);
      STAMP(13 + ps);
#pragma unroll
      for (int i = 0; i < (ps ? NB3 : NB2); i++) LD_WC(pa + 2 * NB0 + (ps ? 0 : NB1) + i);
      {
        const float s_ = dot2(p0, ONES, dot2(p1, ONES, 0.f)), ss_ = dot2(p0, p0, dot2(p1, p1, 0.f));
        *(f2*)(sm + L_XN + ((c4 * 4 + g4) * 32 + 16 * th + c16) * 16 + ps * 8) = f2{s_, ss_};
      }
      SB();
    });
    v4u wd[28], av[14];
    const int rd = ROTD ? 2 * wrap(wrap(T + 2 * (2 * th2 + a), 7), 7) : 0;
    const int wd_off = OFF_WD + ((2 * T) * 112 + 14 * w) * 1024;
#define LD_WD(i) wd[i] = ldf(ws, (ABL & 1) ? OOB : lo, wd_off + (((i) & 1) * 112 + wrap((((i) >> 2) << 1) + rd, 14)) * 1024, ((i) >> 1) & 1)
#define LD_AV(i, off) av[i] = ldf(ws, (ABL & 4) ? OOB : lo, (off) + wrap(((i) & ~1) + rd, 14) * 1024, (i) & 1)
    const int vs = w < 7 ? l16 : OOB, hso = OFF_HS + tC * 8192 + w * 1024;
    float c1[4], vv[4]; unsigned mneg[4];
    f4 sq[7];
    auto stats_read = [&] {
      const int hsr = fresh(L_HST + l16);
#pragma unroll
      for (int i = 0; i < 7; i++) sq[i] = *(const f4*)(sm + hsr + i * 1024);
    };
    float snm[2], sve[2];
    auto stats_math = [&](int st) {
      if (st < 3) {
#pragma unroll
        for (int c = 0; c < 4; c++) { sq[0][c] += sq[2 * st + 1][c]; PIN(sq[0][c]); }
#pragma unroll
        for (int c = 0; c < 4; c++) { sq[0][c] += sq[2 * st + 2][c]; PIN(sq[0][c]); }
      } else if (st == 3) {
#pragma unroll
        for (int ps = 0; ps < 2; ps++) { snm[ps] = sq[0][2 * ps] * (-1.f / D); PIN(snm[ps]); sve[ps] = fmaxf(sq[0][2 * ps + 1] * (1.f / D) - snm[ps] * snm[ps], 0.f) + 1e-5f; PIN(sve[ps]); }
      } else {
        const int ps = (st - 4) >> 1;
        if (!(st & 1)) {
          const float cc = __builtin_amdgcn_rsqf(sve[ps]) * -1.44269504089f;
          const auto s2 = __builtin_amdgcn_permlane32_swap(__float_as_uint(sve[ps]), __float_as_uint(sve[ps]), false, false);
          const auto s1 = __builtin_amdgcn_permlane32_swap(__float_as_uint(cc), __float_as_uint(cc), false, false);
#pragma unroll
          for (int k = 0; k < 2; k++) { c1[2 * ps + k] = __uint_as_float(s1[k]); vv[2 * ps + k] = __uint_as_float(s2[k]); PIN(c1[2 * ps + k]); PIN(vv[2 * ps + k]); }
        } else {
          const unsigned mh = pk2(snm[ps], 0.f), mm = pk2(snm[ps], snm[ps] - bflo(mh));
          const auto s3 = __builtin_amdgcn_permlane32_swap(mm, mm, false, false);
#pragma unroll
          for (int k = 0; k < 2; k++) { mneg[2 * ps + k] = s3[k]; PIN(mneg[2 * ps + k]); }
        }
      }
    };
    lds_arrive(sm, L_MISC + 40, l);
    {
      unsigned mm = 0;
#pragma unroll
      for (int i = 0; i < 7; i++) { lds_wr(sm, L_H0 + l16 + (7 * w + i) * 1024, hv[i]); mm = mx4(mm, hv[i]); }
      if (bad(mm)) {
        DBG(6);
        poll71(hv, ws, l16, hoff, OOB, 0, 6 DBGP);
#pragma unroll
        for (int i = 0; i < 7; i++) lds_wr(sm, L_H0 + l16 + (7 * w + i) * 1024, hv[i]);
      }
    }
    STAMP(15);
    if (w == 0) {
      lds_wait(lbase + L_MISC + 40, 8u);
      f4 t = {};
#pragma unroll
      for (int i = 0; i < 8; i++) t += *(const f4*)(sm + L_XN + i * 1024 + l16);
#pragma unroll
      for (int i = 0; i < 4; i++) t[i] = sum32(t[i]);
      stc(canon(__builtin_bit_cast(v4u, t)), ws, l < 32 ? l16 + OFF_HS + ((tB * 8 + T) * 2 + a) * 512 : OOB);
    }
    BAR();
    STAMP(16);
    const int ftt = 4 * tB + a + 2 * (w >> 2);
    v4u res;
    const int a0off = OFF_ACT + ((4 * tB + a) * 112 + 14 * w) * 1024;
    const int fdo = (56 + (r >> 2)) * 128 + (l & 3) * 4;
    const int fds = __builtin_amdgcn_readfirstlane(OFF_FD + xcd * 8192);
    const int fao = hh ? fds - 4096 + fdo : OFF_FA + tB * 2048 + (l < 14 ? l * 128 : 0);
    unsigned cs2[2];
    const int co = (4 * hh + (g4 & 1)) * 7168 + c16 * 8, cso = OFF_CS + (128 * cC + 32 * (w >> 1) + (w & 1)) * 4;
    auto make_cs = [&] {
      const float cs = sum32(__uint_as_float(cs2[0] & ~0xffu) + __uint_as_float(cs2[1] & ~0xffu));
      const unsigned hi = pk2(cs, cs), lw = pk2(cs - bflo(hi), 0.f);
      wc[28] = v4u{hh ? 0u : hi, hh ? 0u : lw, 0u, 0u};
    };
    f16v ac[2][2] = {};
    v4u hq[3][2];
    v4u af;
    auto epi = [&](auto PS, auto EE) {
      constexpr int ps = decltype(PS)::v, e = decltype(EE)::v, k = e >> 2, j = 2 * (e & 3);
      if constexpr (e == 0) asm volatile("s_nop 15");
      af[e & 3] = swiglu2(ac[ps][k][j], ac[ps][k][j + 1], ac[ps][k][8 + j], ac[ps][k][9 + j], c1[2 * ps + k], vv[2 * ps + k]);
      if constexpr ((e & 3) == 3) stc(af, ws, l16 + OFF_ACT + ((4 * tC + 2 * ps + k) * 112 + 8 * cC + w) * 1024);
    };
    auto fold_mean = [&](int ps) {
#pragma unroll
      for (int k = 0; k < 2; k++) { const v4u bx = {mneg[2 * ps + k], mneg[2 * ps + k] & 0xffffu, 0u, 0u}; ac[ps][k] = mfma32(wc[28], bx, ac[ps][k]); }
    };
    sfor<0, 2>([&](auto PS) {
      constexpr int ps = decltype(PS)::v;
      const int lo = fresh(l16);
      const int hb = fresh((ps ? L_H1 : L_H0) + l16), hn = fresh(L_H1 + l16);
      unsigned fa = 0, mm = 0, slow = 0;
      const int h1w = fresh(L_H1 + 7 * w * 1024 + l16), hsw = L_HST + w * 1024 + l16;
      if constexpr (ps == 0) {
#pragma unroll
        for (int i = 0; i < 2; i++) { hq[i][0] = lds_rd(sm, hb + i * 1024); hq[i][1] = lds_rd(sm, hb + (28 + i) * 1024); }
      } else { fold_mean(0); fold_mean(1); }
      SB();
      sfor<0, 28>([&](auto II) {
        constexpr int i = decltype(II)::v;
        if constexpr (i % 7 == 0) __builtin_amdgcn_s_setprio(3 - i / 7);
        if constexpr (i % 4 == 0) STAMPX(1 + ps, 1 + i / 4);
        sfor<0, 2>([&](auto KK) {
          constexpr int k = decltype(KK)::v;
          if constexpr (i + 2 < 28) hq[(ps + i + 2) % 3][k] = lds_rd(sm, hb + (k * 28 + i + 2) * 1024);
          else if constexpr (ps == 0) hq[(i + 2) % 3][k] = lds_rd(sm, hn + (k * 28 + i - 26) * 1024);
          ac[ps][k] = mfma32(wc[i], hq[(ps + i) % 3][k], ac[ps][k]); PIN(ac[ps][k]); SB();
          if constexpr (ps == 0 && k == 0 && i >= JAT - 15 && i < JAT && ((JAT - i) & 1)) {
            constexpr int f = (i - (JAT - 15)) / 2;
            mm = mx4(mm, hv[f]);
            lds_wr(sm, f < 7 ? h1w + f * 1024 : hsw, hv[f]);
          }
          if constexpr (ps == 0 && i >= JAT + 2 && i < JAT + 6) stats_math(2 * (i - JAT - 2) + k);
          if constexpr (ps == 1 && i >= 1 && i < 5) epi(IC<0>{}, IC<2 * (i - 1) + k>{});
          SB();
        });
        if constexpr (ps == 0) {
          if constexpr (i < 6) hv[i] = ldf(ws, lo, hoff + 56 * 1024, i);
          if constexpr (i == 11) hv[6] = ldf(ws, lo, hoff + 56 * 1024, 6);
          if constexpr (i >= 6 && NPFB - 6 + i < 28) LD_WC(NPFB - 6 + i);
          if constexpr (i == 12) hv[7] = ldf(ws, vs, hso, 0);
          if constexpr (i == 0) { cs2[0] = __builtin_amdgcn_raw_buffer_load_b32(ws, co, cso, SC1); cs2[1] = __builtin_amdgcn_raw_buffer_load_b32(ws, co, cso + 14336, SC1); }
          if constexpr (i == 14) { mm = (((cs2[0] ^ tag) | (cs2[1] ^ tag)) & 0xffu) ? SENT : mm; make_cs(); }
          if constexpr (i == 10) fa = __builtin_amdgcn_raw_buffer_load_b32(ws, fdo, fds, SC1);
          if constexpr (i == 27 - NWD0) { if (__builtin_amdgcn_ballot_w64(fa != tag) != 0) DBG(2); wait_words(fa, tag, srd, fdo, fds); }
          if constexpr (i >= 28 - NWD0) LD_WD(i - (28 - NWD0));
          if constexpr (i == JAT - 1) *(unsigned*)(sm + L_MISC + (mm == SENT ? 12 : 16)) = 1u;
          if constexpr (i == JAT) {
            SB(); asm volatile("s_waitcnt lgkmcnt(2)\n\ts_barrier" ::: "memory"); SB();
            stats_read(); slow = *(const unsigned*)(sm + L_MISC + 12);
          }
          SB();
        } else {
          if constexpr (NWD0 + i < 28) LD_WD(NWD0 + i);
          if constexpr (i == 20) res = ldf(ws, (l & 7) * 512 + (l >> 3) * 16, OFF_HF + (ftt * 28 + 4 * T) * 1024 + (w & 3) * 128, 0);
          if constexpr (i == J2AT) {
            SB(); asm volatile("s_waitcnt vmcnt(%0)\n\ts_barrier" :: "i"(J2AT - 3) : "memory"); SB();
            store_if(w, tag, srd, l == 0 ? 0 : OOB, OFF_FA + (tC * 16 + cC) * 128);
          }
          if constexpr (i == FAAT - 5) fa = __builtin_amdgcn_raw_buffer_load_b32(ws, fao, 0, SC1);
          if constexpr (i == FAAT) {
            if (__builtin_amdgcn_ballot_w64(fa != tag) != 0) DBG(10);
            wait_words(fa, tag, srd, fao, sfresh(0));
          }
          if constexpr (i >= FAAT) LD_AV(i - FAAT, a0off);
          SB();
        }
      });
      if constexpr (ps == 0) {
        if (__builtin_amdgcn_readfirstlane(slow) != 0u) {
          DBG(7);
          BAR();
          poll71(hv, ws, l16, hoff + 56 * 1024, vs, hso, 8 DBGP);
#pragma unroll
          for (int f = 0; f < 8; f++) lds_wr(sm, f < 7 ? h1w + f * 1024 : hsw, hv[f]);
#pragma nounroll
          for (int spin = 0; spin < SPIN_CAP; spin++) {
            MEMBAR();
            cs2[0] = __builtin_amdgcn_raw_buffer_load_b32(ws, co, cso, SC1); cs2[1] = __builtin_amdgcn_raw_buffer_load_b32(ws, co, cso + 14336, SC1);
            if (__builtin_amdgcn_ballot_w64((((cs2[0] ^ tag) | (cs2[1] ^ tag)) & 0xffu) != 0u) == 0) break;
          }
          make_cs();
          BAR();
          stats_read();
#pragma unroll
          for (int st = 0; st < 8; st++) stats_math(st);
#pragma unroll
          for (int i = 0; i < 2; i++) { hq[1 + i][0] = lds_rd(sm, hn + i * 1024); hq[1 + i][1] = lds_rd(sm, hn + (28 + i) * 1024); }
        }
      }
      STAMP(17 + 2 * ps);
    });
    {
      int lo = fresh(l16);
      v4u av2[14];
      const rsrc_t os2 = mk(p.out, S * D * 2);
      const int oo = (ftt * 32 + 8 * (w & 3) + (l >> 3)) * 896 + 128 * T + (l & 7) * 16;
      int rw[2];
#pragma unroll
      for (int e = 0; e < 2; e++) rw[e] = w * 8192 + hh * 512 + (r & 16) * 16 + ((r + 8 * e + 4 * hh) & 15) * 16;
      const int tk = 8 * (w & 3) + (l >> 3), rr = (l & 7) * 512 + (tk & 16) * 16 + ((tk + 4 * (l & 3)) & 15) * 16;
#define LD_AV2(i) av2[i] = ldf(ws, lo, a0off + 2 * 112 * 1024 + wrap(((i) & ~1) + rd, 14) * 1024, (i) & 1)
      {
        f16v acc0 = {}, acc1 = {};
        sfor<0, 14>([&](auto II) {
          constexpr int i = decltype(II)::v;
          if constexpr (i % 4 == 0) __builtin_amdgcn_s_setprio(3 - i / 4);
          if constexpr (28 - FAAT + 2 * i < 14) LD_AV(28 - FAAT + 2 * i, a0off);
          if constexpr (28 - FAAT + 2 * i + 1 < 14) LD_AV(28 - FAAT + 2 * i + 1, a0off);
          acc0 = mfma32(wd[2 * i], av[i], acc0); PIN(acc0); SB();
          if constexpr (i < 4) epi(IC<1>{}, IC<2 * i>{});
          SB();
          acc1 = mfma32(wd[2 * i + 1], av[i], acc1); PIN(acc1); SB();
          if constexpr (i < 4) epi(IC<1>{}, IC<2 * i + 1>{});
          SB();
          if constexpr (i == 3) { STAMP(20); asm volatile("s_barrier" ::: "memory"); SB(); }
          if constexpr (i >= AV2AT) { LD_AV2(i - AV2AT); SB(); }
        });
        STAMP(21);
        sfor<0, 8>([&](auto QQ) {
          constexpr int q = decltype(QQ)::v;
          if constexpr (14 - AV2AT + q < 14) { LD_AV2(14 - AV2AT + q); }
          const f16v& a_ = q < 4 ? acc0 : acc1;
          lds_wr(sm, L_H0 + rw[q & 1] + q * 1024, f4u(a_[4 * (q & 3)], a_[4 * (q & 3) + 1], a_[4 * (q & 3) + 2], a_[4 * (q & 3) + 3]));
          SB();
        });
#pragma unroll
        for (int i = 22 - AV2AT; i < 14; i++) LD_AV2(i);
        SB();
      }
      {
        f16v acc0 = {}, acc1 = {};
        sfor<0, 14>([&](auto II) {
          constexpr int i = decltype(II)::v;
          if constexpr (i % 4 == 0) __builtin_amdgcn_s_setprio(3 - i / 4);
          acc0 = mfma32(wd[2 * i], av2[i], acc0); acc1 = mfma32(wd[2 * i + 1], av2[i], acc1);
          SB();
        });
        if (__builtin_amdgcn_ballot_w64(acc0[0] != acc0[0]) != 0) {
          DBG(11);
#pragma nounroll
          for (int spin = 0; spin < SPIN_CAP; spin++) {
            MEMBAR();
            unsigned mm = 0;
            lo = fresh(l16);
#pragma unroll
            for (int i = 0; i < 14; i++) LD_AV2(i);
#pragma unroll
            for (int i = 0; i < 14; i++) mm = mx4(mm, av2[i]);
            if (!bad(mm)) break;
          }
          acc0 = f16v{}; acc1 = f16v{};
#pragma unroll
          for (int i = 0; i < 14; i++) { acc0 = mfma32(wd[2 * i], av2[i], acc0); acc1 = mfma32(wd[2 * i + 1], av2[i], acc1); }
        }
        STAMP(22);
        const int r0 = fresh(L_H1 + rw[0]), r1 = fresh(L_H1 + rw[1]);
#pragma unroll
        for (int q = 0; q < 4; q++) {
          lds_wr(sm, ((q & 1) ? r1 : r0) + q * 1024, f4u(acc0[4 * q], acc0[4 * q + 1], acc0[4 * q + 2], acc0[4 * q + 3]));
          lds_wr(sm, ((q & 1) ? r1 : r0) + (4 + q) * 1024, f4u(acc1[4 * q], acc1[4 * q + 1], acc1[4 * q + 2], acc1[4 * q + 3]));
        }
        BAR();
        PIN(res);
        __builtin_amdgcn_raw_buffer_store_b128(red_final<16>(sm, fresh((w < 4 ? L_H0 : L_H1) + rr), res), os2, oo, 0, OAUX);
      }
    }
  }
  asm volatile(".Lfb_end:");
  STAMP(23);
}

extern "C" void kernel_launch(void* const* d_in, const int* in_sizes, int n_in, void* d_out, int out_size, void* d_ws, size_t ws_size, hipStream_t stream) {
  Ptrs p;
  p.x = d_in[0]; p.wqkv = d_in[1]; p.wo = d_in[2]; p.wg = d_in[4]; p.wu = d_in[5]; p.wd = d_in[6]; p.out = (char*)d_out; p.ws = (char*)d_ws;
  fused_block<<<256, 512, 0, stream>>>(p);
}
```

```cpp
#include <hip/hip_runtime.h>
#include <stdint.h>

constexpr int S = 2048, D = 448, F = 1792;
typedef unsigned v4u __attribute__((ext_vector_type(4)));
typedef unsigned v2u __attribute__((ext_vector_type(2)));
typedef float f4 __attribute__((ext_vector_type(4)));
typedef float f2 __attribute__((ext_vector_type(2)));
typedef float f16v __attribute__((ext_vector_type(16)));
typedef __bf16 bf2 __attribute__((ext_vector_type(2)));
typedef __bf16 bf8 __attribute__((ext_vector_type(8)));
typedef int srd_t __attribute__((ext_vector_type(4)));
typedef __amdgpu_buffer_rsrc_t rsrc_t;
#define DI __device__ __forceinline__
#define XNT ""
#define WNT ""
#define SB() __builtin_amdgcn_sched_barrier(0)
#define MEMBAR() asm volatile("" ::: "memory")
#define BAR() do { SB(); asm volatile("s_waitcnt lgkmcnt(0)\n\ts_barrier" ::: "memory"); SB(); } while (0)

constexpr int OFF_EPOCH = 0, OFF_FA = 32768;
constexpr int OFF_FS = 65536, OFF_CS = OFF_FS + 65536  , OFF_FD = OFF_CS + 65536;
constexpr int OFF_WC = 262144;
constexpr int OFF_WD = OFF_WC + 112 * 29 * 1024;
constexpr int OFF_PART = OFF_WD + 1568 * 1024;
constexpr int OFF_KVF = OFF_PART + 2048 * 1024;
constexpr int OFF_PQ = OFF_KVF + 32 * 1024;
constexpr int OFF_HF = OFF_PQ + 2048 * 1024;
constexpr int OFF_HS = OFF_HF + 1792 * 1024;
constexpr int OFF_ACT = OFF_HS + 128 * 1024;
constexpr int WS_BYTES = OFF_ACT + 7168 * 1024;
constexpr int OOB = 0x40000000;
constexpr int SC1 = 16;
constexpr unsigned SENT = 0xFFFFFFFFu;
constexpr int SPIN_CAP = 1 << 16;

#define STAMP(i)
#define STAMPX(set, i)
#define DBG(i)
#define DBGA
#define DBGP

DI rsrc_t mk(const void* p, int bytes) { return __builtin_amdgcn_make_buffer_rsrc((void*)p, 0, bytes, 0x00020000); }
DI v4u ldf(rsrc_t r, int voff, int base, int i) { return __builtin_amdgcn_raw_buffer_load_b128(r, voff + (i & 3) * 1024, base + (i >> 2) * 4096, SC1); }
DI void stc(v4u v, rsrc_t r, int off) { __builtin_amdgcn_raw_buffer_store_b128(v, r, off, 0, SC1); }
DI v4u canon(v4u v) { v4u o; for (int i = 0; i < 4; i++) o[i] = min(v[i], SENT - 1u); return o; }
DI f16v mfma32(v4u a, v4u b, f16v c) { return __builtin_amdgcn_mfma_f32_32x32x16_bf16(__builtin_bit_cast(bf8, a), __builtin_bit_cast(bf8, b), c, 0, 0, 0); }
DI f4 mfma16(v4u a, v4u b, f4 c) { return __builtin_amdgcn_mfma_f32_16x16x32_bf16(__builtin_bit_cast(bf8, a), __builtin_bit_cast(bf8, b), c, 0, 0, 0); }
DI unsigned pk2(float lo, float hi) { f2 v = {lo, hi}; return __builtin_bit_cast(unsigned, __builtin_convertvector(v, bf2)); }
DI float dot2(unsigned a, unsigned b, float c) { return __builtin_amdgcn_fdot2_f32_bf16(__builtin_bit_cast(bf2, a), __builtin_bit_cast(bf2, b), c, false); }
DI float bflo(unsigned u) { return __uint_as_float(u << 16); }
DI float bfhi(unsigned u) { return __uint_as_float(u & 0xffff0000u); }
DI float phi(float t) { return (fmaxf(t, 0.f) + 1e-6f) + __builtin_amdgcn_fmed3f(__builtin_amdgcn_exp2f(t * 1.44269504089f), 0.f, 1.f); }
constexpr unsigned ONES = 0x3f803f80u;
DI v4u lds_rd(const char* sm, int off) { return *(const v4u*)(sm + off); }
DI void lds_wr(char* sm, int off, v4u v) { *(v4u*)(sm + off) = v; }
DI v4u f4u(float a, float b, float c, float d) { v4u r = {__float_as_uint(a), __float_as_uint(b), __float_as_uint(c), __float_as_uint(d)}; return r; }
DI unsigned mx4(unsigned m, v4u v) { return max(max(max(m, v[0]), v[1]), max(v[2], v[3])); }
DI bool bad(unsigned m) { return __builtin_amdgcn_ballot_w64(m == SENT) != 0; }
DI float sum16(float v) { auto s = __builtin_amdgcn_permlane16_swap(__float_as_uint(v), __float_as_uint(v), false, false); return __uint_as_float(s[0]) + __uint_as_float(s[1]); }
DI float sum32(float v) { auto s = __builtin_amdgcn_permlane32_swap(__float_as_uint(v), __float_as_uint(v), false, false); return __uint_as_float(s[0]) + __uint_as_float(s[1]); }
template <int CTRL> DI float dpp_add(float v) { return v + __builtin_bit_cast(float, __builtin_amdgcn_update_dpp(0, __builtin_bit_cast(int, v), CTRL, 0xf, 0xf, true)); }
DI float wave_sum(float v) { v = dpp_add<0xB1>(v); v = dpp_add<0x4E>(v); v = dpp_add<0x141>(v); v = dpp_add<0x140>(v); return sum32(sum16(v)); }
DI int fresh(int v) { asm volatile("" : "+v"(v)); return v; }
#define LIT(x) sfresh(x)
#define PIN(x) asm volatile("" : "+v"(x))
DI unsigned swiglu2(float g0, float g1, float u0, float u1, float c, float v) {
  float t0, t1, p0, p1; unsigned r;
  asm volatile("v_mul_f32 %0, %5, %9\n\tv_mul_f32 %1, %6, %9\n\tv_exp_f32 %0, %0\n\tv_exp_f32 %1, %1\n\t"
               "v_mul_f32 %2, %5, %7\n\tv_mul_f32 %3, %6, %8\n\tv_fma_f32 %0, %0, %10, %10\n\tv_fma_f32 %1, %1, %10, %10\n\t"
               "v_rcp_f32 %0, %0\n\tv_rcp_f32 %1, %1\n\tv_mul_f32 %2, %2, %0\n\tv_mul_f32 %3, %3, %1\n\t"
               "v_cvt_pk_bf16_f32 %4, %2, %3\n\tv_min_u32 %4, -2, %4"
               : "=&v"(t0), "=&v"(t1), "=&v"(p0), "=&v"(p1), "=&v"(r) : "v"(g0), "v"(g1), "v"(u0), "v"(u1), "v"(c), "v"(v));
  return r;
}
DI int sfresh(int v) { asm volatile("" : "+s"(v)); return v; }
DI int wrap(int v, int n) { return v >= n ? v - n : v; }
template <int I> struct IC { static constexpr int v = I; };
template <int I, int N, class Fn> DI void sfor(Fn&& f) { if constexpr (I < N) { f(IC<I>{}); sfor<I + 1, N>(f); } }

struct Ptrs { const void *x, *wqkv, *wo, *wg, *wu, *wd; char* out; char* ws; };

DI srd_t mks(const void* p, int bytes) { return srd_t{(int)(size_t)p, (int)((size_t)p >> 32) & 0xffff, bytes, 0x00020000}; }
DI v4u tag4(f4 v, unsigned tag) { v4u o; for (int i = 0; i < 4; i++) o[i] = __builtin_amdgcn_perm(__float_as_uint(v[i]), tag, 0x07060500u); return o; }
DI unsigned tagx(unsigned m, v4u v, unsigned tag) { return m | ((v[0] ^ tag) | (v[1] ^ tag)) | ((v[2] ^ tag) | (v[3] ^ tag)); }
DI unsigned lo2(unsigned a, unsigned b) { return __builtin_amdgcn_perm(b, a, 0x05040100u); }
DI unsigned hi2(unsigned a, unsigned b) { return __builtin_amdgcn_perm(b, a, 0x07060302u); }
DI void wait_flags(rsrc_t ws, int off, unsigned long long lines, int l, unsigned tag, int id DBGA) {
  const int vo = ((lines >> l) & 1) ? l * 128 : OOB;
#pragma nounroll
  for (int spin = 0; spin < SPIN_CAP; spin++) {
    MEMBAR();
    const v4u f = ldf(ws, vo, off, 0);
    if ((__builtin_amdgcn_ballot_w64(((f[0] ^ tag) | (f[1] ^ tag) | (f[2] ^ tag) | (f[3] ^ tag)) != 0u) & lines) == 0) break;
    DBG(id);
    __builtin_amdgcn_s_sleep(2);
  }
}
DI void set_flag(rsrc_t ws, int off, int b, bool on, int l, unsigned tag) {
  __builtin_amdgcn_raw_buffer_store_b32(tag, ws, (on && l < 8) ? l * 8192 : OOB, off + (b >> 2) * 128 + (b & 3) * 4, SC1);
}
DI void wait_words(unsigned v, unsigned tag, srd_t srd, int voff, int soff) {
  int cnt;
  asm volatile("s_mov_b32 %1, 0x10000\n"
    "1:\n\tv_cmp_ne_u32 vcc, %0, %2\n\ts_cbranch_vccz 2f\n\t"
    "buffer_load_dword %0, %3, %4, %5 offen sc1\n\ts_waitcnt vmcnt(0)\n\t"
    "s_sub_u32 %1, %1, 1\n\ts_cbranch_scc0 1b\n"
    "2:" : "+v"(v), "=&s"(cnt) : "v"(tag), "v"(voff), "s"(srd), "s"(soff) : "vcc", "scc", "memory");
}
template <int N> DI void poll_frags(v4u (&v)[N], rsrc_t ws, int voff, int base, int id DBGA) {
#pragma nounroll
  for (int spin = 0; spin < SPIN_CAP; spin++) {
    MEMBAR();
    const int lo = fresh(voff);
    unsigned m = 0;
#pragma unroll
    for (int i = 0; i < N; i++) v[i] = ldf(ws, lo, base, i);
#pragma unroll
    for (int i = 0; i < N; i++) m = mx4(m, v[i]);
    if (!bad(m)) break;
    DBG(id);
  }
}
DI void poll71(v4u (&v)[8], rsrc_t ws, int voff, int s0, int v1, int s1, int id DBGA) {
#pragma nounroll
  for (int spin = 0; spin < SPIN_CAP; spin++) {
    MEMBAR();
    const int lo = fresh(voff);
    unsigned m = 0;
#pragma unroll
    for (int i = 0; i < 7; i++) v[i] = ldf(ws, lo, s0, i);
    v[7] = ldf(ws, v1, s1, 0);
#pragma unroll
    for (int i = 0; i < 8; i++) m = mx4(m, v[i]);
    if (!bad(m)) break;
    DBG(id);
  }
}
DI void store_if(int c, unsigned v, srd_t srd, int voff, int soff) {
  asm volatile("s_cmp_lg_u32 %0, 0\n\ts_cbranch_scc1 1f\n\tbuffer_store_dword %1, %2, %3, %4 offen sc1\n1:" :: "s"(c), "v"(v), "v"(voff), "s"(srd), "s"(soff) : "scc", "memory");
}
template <class Fn> DI void poll8(v4u (&v)[8], rsrc_t ws, int voff, int s0, int s1, int id DBGA, Fn&& hook) {
  {
    const int lo = fresh(voff);
    unsigned m = 0;
#pragma unroll
    for (int i = 0; i < 8; i++) v[i] = ldf(ws, lo, i < 4 ? s0 : s1, i & 3);
    SB(); hook(); SB();
#pragma unroll
    for (int i = 0; i < 8; i++) m = mx4(m, v[i]);
    if (!bad(m)) return;
    DBG(id);
  }
#pragma nounroll
  for (int spin = 0; spin < SPIN_CAP; spin++) {
    MEMBAR();
    const int lo = fresh(voff);
    unsigned m = 0;
#pragma unroll
    for (int i = 0; i < 8; i++) v[i] = ldf(ws, lo, i < 4 ? s0 : s1, i & 3);
#pragma unroll
    for (int i = 0; i < 8; i++) m = mx4(m, v[i]);
    if (!bad(m)) break;
    DBG(id);
  }
}
typedef short s4v __attribute__((ext_vector_type(4)));
#define LAS __attribute__((address_space(3)))
DI v4u tr_frag(const char* sm, int off) {
  const v2u a = __builtin_bit_cast(v2u, __builtin_amdgcn_ds_read_tr16_b64_v4i16((LAS s4v*)((LAS char*)sm + off)));
  const v2u b = __builtin_bit_cast(v2u, __builtin_amdgcn_ds_read_tr16_b64_v4i16((LAS s4v*)((LAS char*)sm + off + 448)));
  return v4u{a[0], a[1], b[0], b[1]};
}
DI void dma_kv(int voff, srd_t srd, int soff, int lds) {
  unsigned keep;
  asm volatile("s_mov_b32 %0, m0\n\ts_mov_b32 exec_hi, 0x00ffffff\n\ts_mov_b32 m0, %4\n\ts_nop 0\n\t"
               "buffer_load_dwordx4 %1, %2, %3 offen offset:896" WNT " lds\n\tbuffer_load_dwordx4 %1, %2, %3 offen offset:1792" WNT " lds\n\t"
               "s_mov_b32 m0, %0\n\ts_mov_b32 exec_hi, -1" : "=&s"(keep) : "v"(voff), "s"(srd), "s"(soff), "s"(lds - 896) : "memory");
}
DI void dma_q(int voff, srd_t srd, int soff, int lds) {
  unsigned keep;
  asm volatile("s_mov_b32 %0, m0\n\ts_mov_b32 exec_hi, 0x00ffffff\n\ts_mov_b32 m0, %4\n\ts_nop 0\n\t"
               "buffer_load_dwordx4 %1, %2, %3 offen" WNT " lds\n\t"
               "s_mov_b32 m0, %0\n\ts_mov_b32 exec_hi, -1" : "=&s"(keep) : "v"(voff), "s"(srd), "s"(soff), "s"(lds) : "memory");
}
DI void lds_arrive(char* sm, int off, int l) { if (l == 0) __hip_atomic_fetch_add((unsigned*)(sm + off), 1u, __ATOMIC_RELAXED, __HIP_MEMORY_SCOPE_WORKGROUP); }
DI void lds_arrive_nb(int addr_lane0, int addr_other, int l) { asm volatile("ds_add_u32 %0, %1" :: "v"(l == 0 ? addr_lane0 : addr_other + l * 4), "v"(1) : "memory"); }
DI void lds_wait(int addr, unsigned n) {
  unsigned t;
  asm volatile("s_branch 2f\n1:\n\ts_sleep 1\n2:\n\tds_read_b32 %0, %1\n\ts_waitcnt lgkmcnt(0)\n\tv_cmp_gt_u32 vcc, %2, %0\n\ts_cbranch_vccnz 1b" : "=&v"(t) : "v"(addr), "s"(n) : "vcc", "memory");
}
DI void dma_full_nt(int voff, srd_t srd, int soff, int lds) {
  unsigned keep;
  asm volatile("s_mov_b32 %0, m0\n\ts_mov_b32 m0, %4\n\ts_nop 0\n\tbuffer_load_dwordx4 %1, %2, %3 offen nt lds\n\ts_mov_b32 m0, %0" : "=&s"(keep) : "v"(voff), "s"(srd), "s"(soff), "s"(lds) : "memory");
}
DI void dma_full(int voff, srd_t srd, int soff, int lds) {
  unsigned keep;
  asm volatile("s_mov_b32 %0, m0\n\ts_mov_b32 m0, %4\n\ts_nop 0\n\tbuffer_load_dwordx4 %1, %2, %3 offen lds\n\ts_mov_b32 m0, %0" : "=&s"(keep) : "v"(voff), "s"(srd), "s"(soff), "s"(lds) : "memory");
}
DI v2u tr_half(const char* sm, int off) { return __builtin_bit_cast(v2u, __builtin_amdgcn_ds_read_tr16_b64_v4i16((LAS s4v*)((LAS char*)sm + off))); }
template <int G> DI v4u red_final(const char* sm, int off, v4u res) {
  f4 e = {bflo(res[0]), bflo(res[1]), bflo(res[2]), bflo(res[3])}, o = {bfhi(res[0]), bfhi(res[1]), bfhi(res[2]), bfhi(res[3])};
#pragma unroll
  for (int g = 0; g < 16; g += G) {
    v4u t[G];
#pragma unroll
    for (int i = 0; i < G; i++) t[i] = lds_rd(sm, off + (g + i) * 4096);
    SB();
#pragma unroll
    for (int w = 0; w < G / 2; w++) { e += __builtin_bit_cast(f4, t[2 * w]); o += __builtin_bit_cast(f4, t[2 * w + 1]); }
    SB();
  }
  v4u out;
#pragma unroll
  for (int i = 0; i < 4; i++) out[i] = pk2(e[i], o[i]);
  return out;
}

constexpr int L_X = 0;
constexpr int L_KV = 57344;
constexpr int L_KVIN = 114688;
constexpr int L_WO = 57344;
constexpr int L_OB0 = 0, L_OB1 = 114688;
constexpr int L_H0 = 0;
constexpr int L_H1 = 65536;
constexpr int L_HST = 131328;
constexpr int L_XN = 149504;
constexpr int L_MISC = 157696;
constexpr int L_DUMMY = 157824;
constexpr int L_CS = 158080;
constexpr int L_BYTES = 162176;

#define ABL 0
#define DLY_X 20
#define DLY_AV 10
#define AV2AT 14
#define WARM 0
#define ROTD 0
#define UNT " nt"
#define DLY_KQ 4
#define J2AT 11
#define JAT 22
#define FAAT 25
#define OAUX 16
#define NB0 2
#define NB1 2
#define NB2 3
#define NB3 2
#define NPFB (NPF1 + 4 * NB0 + NB1 + NB2 + NB3)
#define MAPA 0
#define MAPC 0
#define TW 1
#define SPOS 1
#define SERL 1
#define ROTC 0
#define DLY_PQ 0
#define DLY_KF 4
#define DLY_F1 8
#define DLY_KV 24
#define NPF0 6
#define NPF1 9
#define NPF2 9
#define NPF3 15
#define NWD0 8

__global__ __launch_bounds__(512, 2) void fused_block(Ptrs p) {
  __shared__ __attribute__((aligned(16))) char sm[L_BYTES];
  STAMP(0);
  unsigned ka;
  asm volatile("s_load_dword %0, %1, 0" : "=s"(ka) : "s"(__builtin_amdgcn_kernarg_segment_ptr()) : "memory");
  SB();
  const int b = blockIdx.x, xcd = b & 7, idx = b >> 3;
  const int w = __builtin_amdgcn_readfirstlane(threadIdx.x >> 6), l = threadIdx.x & 63, l16 = l * 16;
  const int t64 = MAPA ? 4 * xcd + (idx & 3) : 8 * (xcd & 3) + (idx & 7), hp = MAPA ? ((idx >> 2) + 1) & 7 : (xcd < 4 ? 3 - (idx >> 3) : 4 + (idx >> 3));
  const int uks = 7 * ((b >> 1) & 3) + w - (w > 0);
  int kc[7];
  {
    const int rot = MAPA ? wrap(2 * (idx & 3) + (idx >> 2), 7) : wrap((idx & 7) + 2 * (idx >> 3), 7);
#pragma unroll
    for (int i = 0; i < 7; i++) kc[i] = sfresh(wrap(i + rot, 7));
  }
  const int uv = fresh((b < 224 && w) ? (l >> 5) * 28672 + (l & 31) * 4 + uks * 57344 + idx * 128 : OOB);
  const int xv = fresh((t64 * 64 + w * 8 + (l >> 3)) * 896 + (l & 7) * 16);
  const int ev = fresh(w == 0 ? 0 : ((w == 4 && l < 32) ? ((idx & 7) * 32 + l) * 128 : ((w == 3 && (TW & 1) && l < 14) ? (b * 14 + l) * 128 : ((w == 2 && (TW & 2) && l < 14) ? (2 * b + (l >= 7)) * 2688 + (l >= 7 ? l - 7 : l) * 128 : OOB))));
  unsigned long long pc; int len;
  asm volatile("s_getpc_b64 %0\n.Lfb_pc:\n\ts_mov_b32 %1, .Lfb_end-.Lfb_pc" : "=s"(pc), "=s"(len));
  asm volatile("s_waitcnt lgkmcnt(0)" : "+s"(ka) :: "memory");
  SB();
  const rsrc_t ws = mk(p.ws, WS_BYTES);
  const srd_t srd = mks(p.ws, WS_BYTES);
  unsigned ep, ut[8];
  v4u xg[7];
  {
    const srd_t us = mks((b & 1) ? p.wu : p.wg, D * F * 2);
#pragma unroll
    for (int j = 0; j < 8; j++) asm volatile("buffer_load_dword %0, %1, %2, %3 offen offset:%4" UNT : "=v"(ut[j]) : "v"(uv), "s"(us), "s"(LIT((j >> 1) * 7168)), "i"((j & 1) * 3584) : "memory");
  }
  asm volatile("buffer_load_dword %0, %1, %2, 0 offen sc1" : "=v"(ep) : "v"(ev), "s"(mks(w == 0 ? (const void*)p.ws : (w == 3 ? p.wo : (w == 2 ? p.wqkv : (const void*)pc)), w == 3 ? D * D * 2 : (w == 2 ? D * 3 * D * 2 : len))) : "memory");
  {
    const srd_t xs = mks(p.x, S * D * 2);
#pragma unroll
    for (int i = 0; i < 7; i++) asm volatile("buffer_load_dwordx4 %0, %1, %2, %3 offen" XNT : "=v"(xg[i]) : "v"(xv), "s"(xs), "s"(kc[i] * 128) : "memory");
  }
  asm volatile("s_barrier" ::: "memory");
  const srd_t wsrd = mks(p.wqkv, D * 3 * D * 2);
  const int l7 = (l * 37) >> 8, wv = l7 * 2688 + (l - 7 * l7) * 16 + hp * 112;
  const int lbase = (int)(size_t)(LAS char*)sm;
#define DMA_KV(c) dma_kv(wv, wsrd, (kc[c] * 8 + w) * 21504, lbase + L_KV + ((c) * 8 + w) * 1792)
#define DMA_Q(c) dma_q(wv, wsrd, (kc[c] * 8 + w) * 21504, lbase + L_X + ((c) * 8 + w) * 896)
  const int th2 = idx >= 14, tB = 2 * xcd + th2, m = idx - 14 * th2, T = m >> 1, a = m & 1;
  const bool ffn = idx < 28;
  const int r = l & 31, c16 = l & 15, g4 = l >> 4, hh = l >> 5;
  const v4u sent = {SENT, SENT, SENT, SENT};
  const int tt = w & 1, hd = (w >> 1) & 1;
  const int pq_off = OFF_PQ + ((2 * t64 + tt) * 16 + 2 * hp + hd) * 2048 + l16;
  const int part_off = OFF_PART + ((2 * hp + hd) * 32 + t64) * 4096 + (w & 1) * 2048 + l16;
  DMA_KV(0);
  SB();
  const int jc = (xcd >> 1) * 28 + idx;
  const int tC = MAPC == 1 ? 8 * (xcd & 1) + (jc & 7) : (MAPC == 2 ? tB : 4 * (xcd & 3) + (idx & 3)), cC = MAPC == 1 ? jc >> 3 : (MAPC == 2 ? m : 7 * (xcd >> 2) + (idx >> 2));
  const int jcs = b - 224;
  *(v4u*)(sm + L_MISC + (threadIdx.x == 0 ? 0 : (threadIdx.x == 1 ? 32 : 16))) = v4u{0u, 0u, 0u, 0u};
  const int d0 = fresh(w ? (ffn ? OFF_WC + (116 * idx + uks) * 1024 + (g4 & 1) * 59392 + hh * 512 + (b & 1) * 256 + c16 * 16 : OOB) : (ffn ? OOB : l16 + OFF_KVF + jcs * 1024));
  const int d1 = fresh(w ? d0 + 29696 : ((ffn && l < 32) ? l16 + OFF_HS + ((tB * 8 + T) * 2 + a) * 512 : OOB));
  const int d2 = fresh(ffn ? l16 + OFF_HF + ((4 * tB + a + 2 * (w >> 2)) * 28 + 4 * T + (w & 3)) * 1024 : OOB);
  asm volatile("s_waitcnt vmcnt(10)" : "+v"(ut[0]), "+v"(ut[1]), "+v"(ut[2]), "+v"(ut[3]), "+v"(ut[4]), "+v"(ut[5]), "+v"(ut[6]), "+v"(ut[7]));
  v4u ue = {lo2(ut[0], ut[1]), lo2(ut[2], ut[3]), lo2(ut[4], ut[5]), lo2(ut[6], ut[7])}, uo_ = {hi2(ut[0], ut[1]), hi2(ut[2], ut[3]), hi2(ut[4], ut[5]), hi2(ut[6], ut[7])};
  {
    f2 cs = {0.f, 0.f};
#pragma unroll
    for (int j = 0; j < 4; j++) { cs[0] = dot2(ue[j], ONES, cs[0]); cs[1] = dot2(uo_[j], ONES, cs[1]); }
    *(f2*)(sm + L_CS + threadIdx.x * 8) = cs;
    ue |= w ? 0u : SENT; uo_ |= w ? 0u : SENT;
  }
  if (SERL) stc(ue, ws, d0);
  SB();
  asm volatile("s_waitcnt vmcnt(%8)" : "+v"(ep), "+v"(xg[0]), "+v"(xg[1]), "+v"(xg[2]), "+v"(xg[3]), "+v"(xg[4]), "+v"(xg[5]), "+v"(xg[6]) : "i"(SERL ? 3 : 2));
  STAMP(1);
  DMA_KV(1);
  if (!SERL) stc(ue, ws, d0);
  SB();
  {
    float s = 0.f, ss = 0.f;
#pragma unroll
    for (int i = 0; i < 7; i++)
#pragma unroll
      for (int j = 0; j < 4; j++) { s = dot2(xg[i][j], ONES, s); ss = dot2(xg[i][j], xg[i][j], ss); }
    SB(); DMA_KV(2); stc(uo_, ws, d1); SB();
    s = dpp_add<0x141>(dpp_add<0x4E>(dpp_add<0xB1>(s))); ss = dpp_add<0x141>(dpp_add<0x4E>(dpp_add<0xB1>(ss)));
    const float mean = s * (1.f / D), var = fmaxf(ss * (1.f / D) - mean * mean, 0.f);
    const float ra = __builtin_amdgcn_rsqf(var + 1e-5f), rb = -mean * ra;
    const int o = L_X + w * 7168 + (l >> 3) * 128 + (((l & 7) ^ (4 * (w & 1) + (l >> 4))) * 16);
#pragma unroll
    for (int i = 0; i < 7; i++) {
      float f[8];
#pragma unroll
      for (int j = 0; j < 4; j++) { f[2 * j] = bflo(xg[i][j]); f[2 * j + 1] = bfhi(xg[i][j]); }
#pragma unroll
      for (int j = 0; j < 8; j++) { f[j] = __builtin_fmaf(f[j], ra, rb); asm volatile("" : "+v"(f[j])); }
      const v4u xn = {pk2(f[0], f[1]), pk2(f[2], f[3]), pk2(f[4], f[5]), pk2(f[6], f[7])};
      lds_wr(sm, o + i * 1024, xn);
      if (i == (SPOS ? 4 : 1)) { SB(); stc(sent, ws, pq_off + (w >> 2) * 1024); SB(); }
      if (i == 3) { SB(); DMA_KV(3); SB(); }
      if (i == 5 && !SPOS) { SB(); stc(sent, ws, d2); SB(); }
    }
  }
  *(unsigned*)(sm + L_MISC + (threadIdx.x == 0 ? 0 : 16)) = ep + 1u;
  SB();
  DMA_KV(4);
  BAR();
  STAMP(2);
  const unsigned tag = __builtin_amdgcn_readfirstlane(*(const unsigned*)(sm + L_MISC));
  SB();
  v4u xf[28];
  {
    const int row = 32 * tt + r, xrb = L_X + (row >> 3) * 7168 + (row & 7) * 128, sw = (row >> 1) & 7;
    int xa[4];
#pragma unroll
    for (int i = 0; i < 4; i++) xa[i] = xrb + (((2 * i + hh) ^ sw) * 16);
#pragma unroll
    for (int i = 0; i < 28; i++) {
      xf[i] = lds_rd(sm, xa[i & 3] + (i >> 2) * 1024);
      if (i == 3 && SPOS) { SB(); stc(sent, ws, d2); SB(); }
      if (i == 9) { SB(); DMA_KV(5); SB(); }
      if (i == 21) { SB(); DMA_KV(6); SB(); }
    }
  }
  const int trl = (c16 >> 2) * 112 + hd * 56 + min(4 * (g4 & 1) + (c16 & 3), 6) * 8;
  f16v acc = {};
  const int ba = fresh(L_KV + (w >> 2) * 896 + hh * 1792 + trl), bb2 = fresh(L_KV + (w >> 2) * 896 + hh * 1792 + trl + 57344);
#define RD_KV(i) tr_frag(sm, (i) < 16 ? ba + (i) * 3584 : bb2 + ((i) - 16) * 3584)
  v4u bb[4];
  {
    SB();
    SB(); asm volatile("s_waitcnt vmcnt(%0) lgkmcnt(0)\n\ts_barrier" :: "i"(SPOS ? 8 : 7) : "memory"); SB();
#pragma unroll
    for (int i = 0; i < 3; i++) bb[i] = RD_KV(i);
    sfor<0, 13>([&](auto II) {
      constexpr int i = decltype(II)::v;
      if constexpr (i % 7 == 0) __builtin_amdgcn_s_setprio(3 - i / 7);
      if constexpr (i == 4 || i == 8 || i == 12) DMA_Q(i / 4 - 1);
      bb[(i + 3) & 3] = RD_KV(i + 3);
      acc = mfma32(xf[i], bb[i & 3], acc);
      SB();
    });
    asm volatile("s_waitcnt vmcnt(3) lgkmcnt(0)\n\ts_barrier" ::: "memory"); SB();
    STAMP(3);
  }
  v4u wc[29];
  const int wc_off = OFF_WC + (8 * cC + w) * 29 * 1024;
#define LD_WC(i) wc[i] = ldf(ws, lo, wc_off, i)
  const srd_t os = mks(ffn ? p.wo : p.wd, ffn ? D * D * 2 : F * D * 2);
  const int ov = ffn ? (l >> 3) * 896 + (((l & 7) ^ (2 * ((l >> 4) & 3))) * 16) + 128 * T : (jcs < 28 ? l16 + jcs * 57344 : OOB), og = ffn ? 14 * (2 * th2 + a) : 0, ost = ffn ? 7168 : 1024;
#define DMA_WO(g) do { const int gg_ = wrap((g) + og, 56); dma_full(ov, os, gg_ * ost, lbase + L_WO + gg_ * 1024); } while (0)
  if (w < 4) {
    __builtin_amdgcn_s_setprio(3);
    sfor<13, 28>([&](auto II) {
      constexpr int i = decltype(II)::v;
      if constexpr (i == 21) { SB(); lds_arrive_nb(lbase + L_MISC + 32, lbase + L_DUMMY, l); SB(); }
      if constexpr (i + 3 < 28) bb[(i + 3) & 3] = RD_KV(i + 3);
      acc = mfma32(xf[i], bb[i & 3], acc);
      SB();
    });
    STAMP(4);
    {
      v4u fr[2];
#pragma unroll
      for (int j = 0; j < 8; j++) fr[j >> 2][j & 3] = pk2(phi(acc[2 * j]), phi(acc[2 * j + 1]));
      SB(); lds_wait(lbase + L_MISC + 32, 8u);
      lds_wr(sm, L_KVIN + w * 2048 + l16, fr[0]); lds_wr(sm, L_KVIN + w * 2048 + 1024 + l16, fr[1]);
    }
    SB(); asm volatile("s_waitcnt vmcnt(0) lgkmcnt(0)\n\ts_barrier" ::: "memory"); SB();
    STAMP(5);
    {
      v4u ka[2], vb[2][2];
      const int ko = L_KVIN + hd * 4096 + g4 * 512 + c16 * 16;
#pragma unroll
      for (int t_ = 0; t_ < 2; t_++) { ka[t_] = lds_rd(sm, ko + t_ * 2048 + (w & 1) * 256); vb[t_][0] = lds_rd(sm, ko + 8192 + t_ * 2048); vb[t_][1] = lds_rd(sm, ko + 8192 + t_ * 2048 + 256); }
      SB();
      f4 kv[2] = {};
#pragma unroll
      for (int t_ = 0; t_ < 2; t_++) { kv[0] = mfma16(ka[t_], vb[t_][0], kv[0]); kv[1] = mfma16(ka[t_], vb[t_][1], kv[1]); }
      stc(tag4(kv[0], tag), ws, part_off); stc(tag4(kv[1], tag), ws, part_off + 1024);
    }
    STAMP(6);
    if (w == 2) {
      float cs = 0.f;
#pragma unroll
      for (int i = 2; i < 16; i++) cs += *(const float*)(sm + L_CS + i * 256 + l * 4);
      __builtin_amdgcn_raw_buffer_store_b32(__builtin_amdgcn_perm(__float_as_uint(cs), tag, 0x07060500u), ws, ffn ? l * 4 : OOB, OFF_CS + (b & 7) * 7168 + idx * 256, SC1);
    }
    if (w == 3 && ffn) {
      wait_flags(ws, OFF_FS + xcd * 8192, ~0ull, l, tag, 0 DBGP);
      if (b == 0 && l == 0) __builtin_amdgcn_raw_buffer_store_b32(tag, mk(p.ws, 64), 0, OFF_EPOCH, SC1);
      lds_arrive(sm, L_MISC + 4, l);
    } else if (ffn) {
      __builtin_amdgcn_s_setprio(0);
#pragma nounroll
      for (int g = w; g < 56; g += 3) DMA_WO(g);
    }
    __builtin_amdgcn_s_setprio(0);
  } else {
    __builtin_amdgcn_s_setprio(0);
    sfor<13, 28>([&](auto II) {
      constexpr int i = decltype(II)::v;
      if constexpr (i == 13) store_if(w - 7, tag, srd, l < 8 ? l * 8192 : OOB, OFF_FS + (b >> 2) * 128 + (b & 3) * 4);
      if constexpr (i == 21) { SB(); lds_arrive_nb(lbase + L_MISC + 32, lbase + L_DUMMY, l); SB(); }
      if constexpr (i == 15 || i == 18 || i == 22 || i == 25) {
        constexpr int c = i == 15 ? 3 : (i == 18 ? 4 : (i == 22 ? 5 : 6));
        dma_q(wv, wsrd, (kc[c] * 8 + w - 4) * 21504, lbase + L_X + (c * 8 + w - 4) * 896); DMA_Q(c);
      }
      if constexpr (i + 3 < 28) bb[(i + 3) & 3] = RD_KV(i + 3);
      acc = mfma32(xf[i], bb[i & 3], acc);
      SB();
    });
    STAMP(4);
    {
      const unsigned pad = r == 28 ? ONES : 0u;
      v4u fr[2];
#pragma unroll
      for (int j = 0; j < 8; j++) { const unsigned t_ = pk2(acc[2 * j], acc[2 * j + 1]); fr[j >> 2][j & 3] = r < 28 ? t_ : pad; }
      SB(); lds_wait(lbase + L_MISC + 32, 8u);
      lds_wr(sm, L_KVIN + w * 2048 + l16, fr[0]); lds_wr(sm, L_KVIN + w * 2048 + 1024 + l16, fr[1]);
    }
    BAR();
    STAMP(5);
    asm volatile("s_waitcnt vmcnt(0)" ::: "memory");
    lds_arrive(sm, L_MISC + 36, l);
    lds_wait(lbase + L_MISC + 36, 4u);
    const int qa = fresh(L_X + hh * 896 + trl);
    f16v aq = {};
#pragma unroll
    for (int i = 0; i < 3; i++) bb[i] = tr_frag(sm, qa + i * 1792);
    SB();
    sfor<0, 28>([&](auto II) {
      constexpr int i = decltype(II)::v;
      if constexpr (i + 3 < 28) bb[(i + 3) & 3] = tr_frag(sm, qa + (i + 3) * 1792);
      aq = mfma32(bb[i & 3], xf[i], aq);
      SB();
    });
    __builtin_amdgcn_s_setprio(3);
#pragma unroll
    for (int s = 0; s < 2; s++) {
      v4u pf = {pk2(phi(aq[8 * s]), phi(aq[8 * s + 1])), pk2(phi(aq[8 * s + 2]), phi(aq[8 * s + 3])), pk2(phi(aq[8 * s + 4]), phi(aq[8 * s + 5])), pk2(phi(aq[8 * s + 6]), phi(aq[8 * s + 7]))};
      if (s == 1) { pf[2] = hh ? 0u : pf[2]; pf[3] = hh ? 0u : pf[3]; }
      stc(canon(pf), ws, pq_off + s * 1024);
    }
    __builtin_amdgcn_s_setprio(0);
    STAMP(8);
  }
  SB();
  STAMP(10);
  if (!ffn) {
    {
      const int rh = jcs >> 1, rj = (jcs & 1) * 8 + w;
      v4u pv[8];
      const int so = OFF_PART + rh * 131072 + ((rj >> 3) * 2 + ((rj >> 1) & 1)) * 1024 + ((rj >> 2) & 1) * 256 + (rj & 1) * 128;
      const unsigned tb = tag & 0xffu;
      if (w < 4) __builtin_amdgcn_s_sleep(DLY_KV); else __builtin_amdgcn_s_sleep(DLY_KQ);
#pragma nounroll
      for (int spin = 0; spin < SPIN_CAP; spin++) {
        MEMBAR();
        const int vo = fresh(g4 * 4096 + (c16 >> 3) * 512 + (c16 & 7) * 16);
        unsigned mm = 0;
#pragma unroll
        for (int i = 0; i < 8; i++) pv[i] = ldf(ws, vo, so + i * 16384, 0);
#pragma unroll
        for (int i = 0; i < 8; i++) { pv[i] ^= tb; mm |= (pv[i][0] | pv[i][1]) | (pv[i][2] | pv[i][3]); }
        if (__builtin_amdgcn_ballot_w64((mm & 0xffu) != 0u) == 0) break;
        DBG(3);
      }
      f4 s = __builtin_bit_cast(f4, pv[0]);
#pragma unroll
      for (int i = 1; i < 8; i++) s += __builtin_bit_cast(f4, pv[i]);
#pragma unroll
      for (int i = 0; i < 4; i++) s[i] = sum32(sum16(s[i]));
      const v2u o = {min(pk2(s[0], s[1]), SENT - 1u), min(pk2(s[2], s[3]), SENT - 1u)};
      __builtin_amdgcn_raw_buffer_store_b64(o, ws, l < 16 ? (c16 & 7) * 16 + (c16 >> 3) * 8 : OOB, OFF_KVF + jcs * 1024 + w * 128, SC1);
      STAMP(9);
      BAR();
    }
#pragma unroll
    for (int i = 0; i < 7; i++) dma_full_nt(ov, os, (8 * i + w) * 1024, lbase + L_WO + (8 * i + w) * 1024);
    const int ao = l16 + OFF_ACT + ((4 * (jcs >> 1) + 2 + (jcs & 1)) * 112 + w) * 1024;
#pragma unroll
    for (int i = 0; i < 5; i++) stc(sent, ws, ao + i * 8192);
    asm volatile("s_waitcnt vmcnt(5)" ::: "memory");
    BAR();
    if (jcs < 28) {
      if (w < 7) {
        unsigned u_[4][8];
        const int dv = fresh(L_WO + hh * 7168 + r * 4 + 128 * w);
#pragma unroll
        for (int u = 0; u < 4; u++)
#pragma unroll
          for (int j = 0; j < 8; j++) u_[u][j] = *(const unsigned*)(sm + dv + (u & 1) * 896 + (u >> 1) * 28672 + (j & 3) * 1792 + (j >> 2) * 14336);
        SB();
#pragma unroll
        for (int u = 0; u < 4; u++) {
          const int d0 = OFF_WD + (224 * w + 4 * jcs + u) * 1024 + l16;
          stc(v4u{lo2(u_[u][0], u_[u][1]), lo2(u_[u][2], u_[u][3]), lo2(u_[u][4], u_[u][5]), lo2(u_[u][6], u_[u][7])}, ws, d0);
          stc(v4u{hi2(u_[u][0], u_[u][1]), hi2(u_[u][2], u_[u][3]), hi2(u_[u][4], u_[u][5]), hi2(u_[u][6], u_[u][7])}, ws, d0 + 114688);
        }
      }
    }
    asm volatile("s_waitcnt vmcnt(0)" ::: "memory");
    BAR();
    if (w == 0) set_flag(ws, OFF_FD, b, true, l, tag);
#pragma unroll
    for (int i = 5; i < 14; i++) stc(sent, ws, ao + i * 8192);
    asm volatile("s_waitcnt vmcnt(0)" ::: "memory");
    BAR();
    if (w == 0) set_flag(ws, OFF_FD - 4096, b, true, l, tag);
  } else {
    const int c4 = w & 3, th = w >> 2;
    v4u wo[16], pq[8], kvf[4], hv[8];
    int woa[2];
#pragma unroll
    for (int e = 0; e < 2; e++) woa[e] = L_WO + (16 * (g4 >> 1) + 4 * (g4 & 1) + (c16 >> 2)) * 128 + (((2 * c4 + ((c16 >> 1) & 1)) ^ (2 * ((((e + g4) & 1) << 1) + (c16 >> 3)))) * 16) + (c16 & 1) * 8;
    const int wo8 = g4 == 3 ? 0 : 1024;
    const int hoff = OFF_HF + (4 * tC * 28 + 7 * w) * 1024;
    v2u xr[2];
    lds_wait(lbase + L_MISC + 4, 1u);
    {
      const int lo = fresh(l16);
#pragma unroll
      for (int ps = 0; ps < 2; ps++) xr[ps] = *(const v2u*)((const char*)p.x + (size_t)((4 * tB + a + 2 * ps) * 32 + 16 * th + c16) * 896 + (64 * T + 16 * c4 + 4 * g4) * 2);
#pragma unroll
      for (int i = 0; i < NPF0; i++) LD_WC(i);
      SB();
      asm volatile("s_waitcnt vmcnt(%0)" :: "i"(NPF0 + 2) : "memory");
      lds_arrive(sm, L_MISC + 8, l);
    }
    lds_wait(lbase + L_MISC + 8, 8u);
    __builtin_amdgcn_s_setprio(2);
    poll8(pq, ws, l16, OFF_PQ + ((4 * tB + a) * 32 + 4 * w) * 1024, OFF_PQ + ((4 * tB + a + 2) * 32 + 4 * w) * 1024, 4 DBGP, [&] {
      const int lo = fresh(l16);
      __builtin_amdgcn_s_setprio(0);
#pragma unroll
      for (int i = 0; i < 16; i++) {
        const v2u x0 = tr_half(sm, woa[i & 1] + i * 3584), x1 = tr_half(sm, woa[i & 1] + wo8 + i * 3584);
        wo[i] = v4u{x0[0], x0[1], x1[0], x1[1]};
      }
      SB();
      if (DLY_KF) __builtin_amdgcn_s_sleep(DLY_KF);
      __builtin_amdgcn_s_setprio(3);
#pragma unroll
      for (int i = 0; i < 4; i++) kvf[i] = ldf(ws, lo, OFF_KVF + 4 * w * 1024, i);
      __builtin_amdgcn_s_setprio(0);
      SB();
#pragma unroll
      for (int i = NPF0; i < NPF1; i++) LD_WC(i);
    });
    STAMP(7);
    {
      unsigned mm = 0;
#pragma unroll
      for (int i = 0; i < 4; i++) mm = mx4(mm, kvf[i]);
      if (bad(mm)) { DBG(5); __builtin_amdgcn_s_setprio(3); poll_frags<4>(kvf, ws, l16, OFF_KVF + 4 * w * 1024, 5 DBGP); __builtin_amdgcn_s_setprio(0); }
    }
    STAMP(11);
    sfor<0, 2>([&](auto PS) {
      constexpr int ps = decltype(PS)::v;
      const int lo = fresh(l16);
      f16v n[2] = {};
#pragma unroll
      for (int s = 0; s < 2; s++)
#pragma unroll
        for (int hd = 0; hd < 2; hd++) n[hd] = mfma32(kvf[2 * hd + s], pq[4 * ps + 2 * hd + s], n[hd]);
      constexpr int pa = ps ? NPF1 + 2 * NB0 + NB1 + NB2 : NPF1;
#pragma unroll
      for (int i = 0; i < NB0; i++) LD_WC(pa + i);
      SB();
#pragma unroll
      for (int hd = 0; hd < 2; hd++) {
        const auto sw = __builtin_amdgcn_permlane32_swap(__float_as_uint(n[hd][12]), __float_as_uint(n[hd][12]), false, false);
        const float inv = __builtin_amdgcn_rcpf(__uint_as_float(sw[1]) + 1e-6f);
        v4u o0, o1;
#pragma unroll
        for (int i = 0; i < 4; i++) { o0[i] = pk2(n[hd][2 * i] * inv, n[hd][2 * i + 1] * inv); o1[i] = pk2(n[hd][8 + 2 * i] * inv, n[hd][9 + 2 * i] * inv); }
        o1[2] = hh ? 0u : o1[2];
        lds_wr(sm, (ps ? L_OB1 : L_OB0) + (4 * w + 2 * hd) * 1024 + lo, o0);
        lds_wr(sm, (ps ? L_OB1 : L_OB0) + (4 * w + 2 * hd + 1) * 1024 + lo, o1);
      }
      SB();
#pragma unroll
      for (int i = 0; i < NB0; i++) LD_WC(pa + NB0 + i);
      BAR();
      if (ps == 0) STAMP(12);
      v4u ob[8];
      const int oo = fresh((ps ? L_OB1 : L_OB0) + g4 * 512 + th * 256 + c16 * 16), vh = fresh(l16);
#pragma unroll
      for (int i = 0; i < 8; i++) ob[i] = lds_rd(sm, oo + i * 2048);
      f4 hacc = {bflo(xr[ps][0]), bfhi(xr[ps][0]), bflo(xr[ps][1]), bfhi(xr[ps][1])};
      SB();
#pragma unroll
      for (int i = 0; i < 16; i++) {
        hacc = mfma16(wo[i], ob[i & 7], hacc);
        if (i < 8) ob[i] = lds_rd(sm, oo + (8 + i) * 2048);
        if (ps == 0 && (i & 3) == 1 && (i >> 2) < NB1) LD_WC(pa + 2 * NB0 + (i >> 2));
        if (ps == 1 && (i & 1) && i < 14) hv[i >> 1] = ldf(ws, vh, hoff, i >> 1);
        SB();
      }
      const unsigned p0 = min(pk2(hacc[0], hacc[1]), SENT - 1u), p1 = min(pk2(hacc[2], hacc[3]), SENT - 1u);
      const auto s0 = __builtin_amdgcn_permlane16_swap(p0, p0, false, false), s1 = __builtin_amdgcn_permlane16_swap(p1, p1, false, false);
      const v4u hf = {s0[0], s1[0], s0[1], s1[1]};
      stc(hf, ws, (g4 & 1) ? OOB : OFF_HF + ((4 * tB + a + 2 * ps) * 28 + 4 * T + c4) * 1024 + (g4 >> 1) * 512 + th * 256 + c16 * 16);
      STAMP(13 + ps);
#pragma unroll
      for (int i = 0; i < (ps ? NB3 : NB2); i++) LD_WC(pa + 2 * NB0 + (ps ? 0 : NB1) + i);
      {
        const float s_ = dot2(p0, ONES, dot2(p1, ONES, 0.f)), ss_ = dot2(p0, p0, dot2(p1, p1, 0.f));
        *(f2*)(sm + L_XN + ((c4 * 4 + g4) * 32 + 16 * th + c16) * 16 + ps * 8) = f2{s_, ss_};
      }
      SB();
    });
    const int ln = fresh(threadIdx.x & 63);
    { const int l = ln, l16 = l * 16, r = l & 31, c16 = l & 15, g4 = l >> 4, hh = l >> 5;
    v4u wd[28], av[14];
    const int rd = ROTD ? 2 * wrap(wrap(T + 2 * (2 * th2 + a), 7), 7) : 0;
    const int wd_off = OFF_WD + ((2 * T) * 112 + 14 * w) * 1024;
#define LD_WD(i) wd[i] = ldf(ws, (ABL & 1) ? OOB : lo, wd_off + (((i) & 1) * 112 + wrap((((i) >> 2) << 1) + rd, 14)) * 1024, ((i) >> 1) & 1)
#define LD_AV(i, off) av[i] = ldf(ws, (ABL & 4) ? OOB : lo, (off) + wrap(((i) & ~1) + rd, 14) * 1024, (i) & 1)
    const int vs = w < 7 ? l16 : OOB, hso = OFF_HS + tC * 8192 + w * 1024;
    float c1[4], vv[4]; unsigned mneg[4];
    f4 sq[7];
    auto stats_read = [&] {
      const int hsr = fresh(L_HST + l16);
#pragma unroll
      for (int i = 0; i < 7; i++) sq[i] = *(const f4*)(sm + hsr + i * 1024);
    };
    float snm[2], sve[2];
    auto stats_math = [&](int st) {
      if (st < 3) {
#pragma unroll
        for (int c = 0; c < 4; c++) { sq[0][c] += sq[2 * st + 1][c]; PIN(sq[0][c]); }
#pragma unroll
        for (int c = 0; c < 4; c++) { sq[0][c] += sq[2 * st + 2][c]; PIN(sq[0][c]); }
      } else if (st == 3) {
#pragma unroll
        for (int ps = 0; ps < 2; ps++) { snm[ps] = sq[0][2 * ps] * (-1.f / D); PIN(snm[ps]); sve[ps] = fmaxf(sq[0][2 * ps + 1] * (1.f / D) - snm[ps] * snm[ps], 0.f) + 1e-5f; PIN(sve[ps]); }
      } else {
        const int ps = (st - 4) >> 1;
        if (!(st & 1)) {
          const float cc = __builtin_amdgcn_rsqf(sve[ps]) * -1.44269504089f;
          const auto s2 = __builtin_amdgcn_permlane32_swap(__float_as_uint(sve[ps]), __float_as_uint(sve[ps]), false, false);
          const auto s1 = __builtin_amdgcn_permlane32_swap(__float_as_uint(cc), __float_as_uint(cc), false, false);
#pragma unroll
          for (int k = 0; k < 2; k++) { c1[2 * ps + k] = __uint_as_float(s1[k]); vv[2 * ps + k] = __uint_as_float(s2[k]); PIN(c1[2 * ps + k]); PIN(vv[2 * ps + k]); }
        } else {
          const unsigned mh = pk2(snm[ps], 0.f), mm = pk2(snm[ps], snm[ps] - bflo(mh));
          const auto s3 = __builtin_amdgcn_permlane32_swap(mm, mm, false, false);
#pragma unroll
          for (int k = 0; k < 2; k++) { mneg[2 * ps + k] = s3[k]; PIN(mneg[2 * ps + k]); }
        }
      }
    };
    lds_arrive(sm, L_MISC + 40, l);
    {
      unsigned mm = 0;
#pragma unroll
      for (int i = 0; i < 7; i++) { lds_wr(sm, L_H0 + l16 + (7 * w + i) * 1024, hv[i]); mm = mx4(mm, hv[i]); }
      if (bad(mm)) {
        DBG(6);
        poll71(hv, ws, l16, hoff, OOB, 0, 6 DBGP);
#pragma unroll
        for (int i = 0; i < 7; i++) lds_wr(sm, L_H0 + l16 + (7 * w + i) * 1024, hv[i]);
      }
    }
    STAMP(15);
    if (w == 0) {
      lds_wait(lbase + L_MISC + 40, 8u);
      f4 t = {};
#pragma unroll
      for (int i = 0; i < 8; i++) t += *(const f4*)(sm + L_XN + i * 1024 + l16);
#pragma unroll
      for (int i = 0; i < 4; i++) t[i] = sum32(t[i]);
      stc(canon(__builtin_bit_cast(v4u, t)), ws, l < 32 ? l16 + OFF_HS + ((tB * 8 + T) * 2 + a) * 512 : OOB);
    }
    BAR();
    STAMP(16);
    const int ftt = 4 * tB + a + 2 * (w >> 2);
    v4u res;
    const int a0off = OFF_ACT + ((4 * tB + a) * 112 + 14 * w) * 1024;
    const int fdo = (56 + (r >> 2)) * 128 + (l & 3) * 4;
    const int fds = __builtin_amdgcn_readfirstlane(OFF_FD + xcd * 8192);
    const int fao = hh ? fds - 4096 + fdo : OFF_FA + tB * 2048 + (l < 14 ? l * 128 : 0);
    unsigned cs2[2];
    const int co = (4 * hh + (g4 & 1)) * 7168 + c16 * 8, cso = OFF_CS + (128 * cC + 32 * (w >> 1) + (w & 1)) * 4;
    auto make_cs = [&] {
      const float cs = sum32(__uint_as_float(cs2[0] & ~0xffu) + __uint_as_float(cs2[1] & ~0xffu));
      const unsigned hi = pk2(cs, cs), lw = pk2(cs - bflo(hi), 0.f);
      wc[28] = v4u{hh ? 0u : hi, hh ? 0u : lw, 0u, 0u};
    };
    f16v ac[2][2] = {};
    v4u hq[3][2];
    v4u af;
    auto epi = [&](auto PS, auto EE) {
      constexpr int ps = decltype(PS)::v, e = decltype(EE)::v, k = e >> 2, j = 2 * (e & 3);
      if constexpr (e == 0) asm volatile("s_nop 15");
      af[e & 3] = swiglu2(ac[ps][k][j], ac[ps][k][j + 1], ac[ps][k][8 + j], ac[ps][k][9 + j], c1[2 * ps + k], vv[2 * ps + k]);
      if constexpr ((e & 3) == 3) stc(af, ws, l16 + OFF_ACT + ((4 * tC + 2 * ps + k) * 112 + 8 * cC + w) * 1024);
    };
    auto fold_mean = [&](int ps) {
#pragma unroll
      for (int k = 0; k < 2; k++) { const v4u bx = {mneg[2 * ps + k], mneg[2 * ps + k] & 0xffffu, 0u, 0u}; ac[ps][k] = mfma32(wc[28], bx, ac[ps][k]); }
    };
    sfor<0, 2>([&](auto PS) {
      constexpr int ps = decltype(PS)::v;
      const int lo = fresh(l16);
      const int hb = fresh((ps ? L_H1 : L_H0) + l16), hn = fresh(L_H1 + l16);
      unsigned fa = 0, mm = 0, slow = 0;
      const int h1w = fresh(L_H1 + 7 * w * 1024 + l16), hsw = L_HST + w * 1024 + l16;
      if constexpr (ps == 0) {
#pragma unroll
        for (int i = 0; i < 2; i++) { hq[i][0] = lds_rd(sm, hb + i * 1024); hq[i][1] = lds_rd(sm, hb + (28 + i) * 1024); }
      } else { fold_mean(0); fold_mean(1); }
      SB();
      sfor<0, 28>([&](auto II) {
        constexpr int i = decltype(II)::v;
        if constexpr (i % 7 == 0) __builtin_amdgcn_s_setprio(3 - i / 7);
        if constexpr (i % 4 == 0) STAMPX(1 + ps, 1 + i / 4);
        sfor<0, 2>([&](auto KK) {
          constexpr int k = decltype(KK)::v;
          if constexpr (i + 2 < 28) hq[(ps + i + 2) % 3][k] = lds_rd(sm, hb + (k * 28 + i + 2) * 1024);
          else if constexpr (ps == 0) hq[(i + 2) % 3][k] = lds_rd(sm, hn + (k * 28 + i - 26) * 1024);
          ac[ps][k] = mfma32(wc[i], hq[(ps + i) % 3][k], ac[ps][k]); PIN(ac[ps][k]); SB();
          if constexpr (ps == 0 && k == 0 && i >= JAT - 15 && i < JAT && ((JAT - i) & 1)) {
            constexpr int f = (i - (JAT - 15)) / 2;
            mm = mx4(mm, hv[f]);
            lds_wr(sm, f < 7 ? h1w + f * 1024 : hsw, hv[f]);
          }
          if constexpr (ps == 0 && i >= JAT + 2 && i < JAT + 6) stats_math(2 * (i - JAT - 2) + k);
          if constexpr (ps == 1 && i >= 1 && i < 5) epi(IC<0>{}, IC<2 * (i - 1) + k>{});
          SB();
        });
        if constexpr (ps == 0) {
          if constexpr (i < 6) hv[i] = ldf(ws, lo, hoff + 56 * 1024, i);
          if constexpr (i == 11) hv[6] = ldf(ws, lo, hoff + 56 * 1024, 6);
          if constexpr (i >= 6 && NPFB - 6 + i < 28) LD_WC(NPFB - 6 + i);
          if constexpr (i == 12) hv[7] = ldf(ws, vs, hso, 0);
          if constexpr (i == 0) { cs2[0] = __builtin_amdgcn_raw_buffer_load_b32(ws, co, cso, SC1); cs2[1] = __builtin_amdgcn_raw_buffer_load_b32(ws, co, cso + 14336, SC1); }
          if constexpr (i == 14) { mm = (((cs2[0] ^ tag) | (cs2[1] ^ tag)) & 0xffu) ? SENT : mm; make_cs(); }
          if constexpr (i == 10) fa = __builtin_amdgcn_raw_buffer_load_b32(ws, fdo, fds, SC1);
          if constexpr (i == 27 - NWD0) { if (__builtin_amdgcn_ballot_w64(fa != tag) != 0) DBG(2); wait_words(fa, tag, srd, fdo, fds); }
          if constexpr (i >= 28 - NWD0) LD_WD(i - (28 - NWD0));
          if constexpr (i == JAT - 1) *(unsigned*)(sm + L_MISC + (mm == SENT ? 12 : 16)) = 1u;
          if constexpr (i == JAT) {
            SB(); asm volatile("s_waitcnt lgkmcnt(2)\n\ts_barrier" ::: "memory"); SB();
            stats_read(); slow = *(const unsigned*)(sm + L_MISC + 12);
          }
          SB();
        } else {
          if constexpr (NWD0 + i < 28) LD_WD(NWD0 + i);
          if constexpr (i == 20) res = ldf(ws, (l & 7) * 512 + (l >> 3) * 16, OFF_HF + (ftt * 28 + 4 * T) * 1024 + (w & 3) * 128, 0);
          if constexpr (i == J2AT) {
            SB(); asm volatile("s_waitcnt vmcnt(%0)\n\ts_barrier" :: "i"(J2AT - 3) : "memory"); SB();
            store_if(w, tag, srd, l == 0 ? 0 : OOB, OFF_FA + (tC * 16 + cC) * 128);
          }
          if constexpr (i == FAAT - 5) fa = __builtin_amdgcn_raw_buffer_load_b32(ws, fao, 0, SC1);
          if constexpr (i == FAAT) {
            if (__builtin_amdgcn_ballot_w64(fa != tag) != 0) DBG(10);
            wait_words(fa, tag, srd, fao, sfresh(0));
          }
          if constexpr (i >= FAAT) LD_AV(i - FAAT, a0off);
          SB();
        }
      });
      if constexpr (ps == 0) {
        if (__builtin_amdgcn_readfirstlane(slow) != 0u) {
          DBG(7);
          BAR();
          poll71(hv, ws, l16, hoff + 56 * 1024, vs, hso, 8 DBGP);
#pragma unroll
          for (int f = 0; f < 8; f++) lds_wr(sm, f < 7 ? h1w + f * 1024 : hsw, hv[f]);
#pragma nounroll
          for (int spin = 0; spin < SPIN_CAP; spin++) {
            MEMBAR();
            cs2[0] = __builtin_amdgcn_raw_buffer_load_b32(ws, co, cso, SC1); cs2[1] = __builtin_amdgcn_raw_buffer_load_b32(ws, co, cso + 14336, SC1);
            if (__builtin_amdgcn_ballot_w64((((cs2[0] ^ tag) | (cs2[1] ^ tag)) & 0xffu) != 0u) == 0) break;
          }
          make_cs();
          BAR();
          stats_read();
#pragma unroll
          for (int st = 0; st < 8; st++) stats_math(st);
#pragma unroll
          for (int i = 0; i < 2; i++) { hq[1 + i][0] = lds_rd(sm, hn + i * 1024); hq[1 + i][1] = lds_rd(sm, hn + (28 + i) * 1024); }
        }
      }
      STAMP(17 + 2 * ps);
    });
    {
      int lo = fresh(l16);
      v4u av2[14];
      const rsrc_t os2 = mk(p.out, S * D * 2);
      const int oo = (ftt * 32 + 8 * (w & 3) + (l >> 3)) * 896 + 128 * T + (l & 7) * 16;
      int rw[2];
#pragma unroll
      for (int e = 0; e < 2; e++) rw[e] = w * 8192 + hh * 512 + (r & 16) * 16 + ((r + 8 * e + 4 * hh) & 15) * 16;
      const int tk = 8 * (w & 3) + (l >> 3), rr = (l & 7) * 512 + (tk & 16) * 16 + ((tk + 4 * (l & 3)) & 15) * 16;
#define LD_AV2(i) av2[i] = ldf(ws, lo, a0off + 2 * 112 * 1024 + wrap(((i) & ~1) + rd, 14) * 1024, (i) & 1)
      {
        f16v acc0 = {}, acc1 = {};
        sfor<0, 14>([&](auto II) {
          constexpr int i = decltype(II)::v;
          if constexpr (i % 4 == 0) __builtin_amdgcn_s_setprio(3 - i / 4);
          if constexpr (28 - FAAT + 2 * i < 14) LD_AV(28 - FAAT + 2 * i, a0off);
          if constexpr (28 - FAAT + 2 * i + 1 < 14) LD_AV(28 - FAAT + 2 * i + 1, a0off);
          acc0 = mfma32(wd[2 * i], av[i], acc0); PIN(acc0); SB();
          if constexpr (i < 4) epi(IC<1>{}, IC<2 * i>{});
          SB();
          acc1 = mfma32(wd[2 * i + 1], av[i], acc1); PIN(acc1); SB();
          if constexpr (i < 4) epi(IC<1>{}, IC<2 * i + 1>{});
          SB();
          if constexpr (i == 3) { STAMP(20); asm volatile("s_barrier" ::: "memory"); SB(); }
          if constexpr (i >= AV2AT) { LD_AV2(i - AV2AT); SB(); }
        });
        STAMP(21);
        sfor<0, 8>([&](auto QQ) {
          constexpr int q = decltype(QQ)::v;
          if constexpr (14 - AV2AT + q < 14) { LD_AV2(14 - AV2AT + q); }
          const f16v& a_ = q < 4 ? acc0 : acc1;
          lds_wr(sm, L_H0 + rw[q & 1] + q * 1024, f4u(a_[4 * (q & 3)], a_[4 * (q & 3) + 1], a_[4 * (q & 3) + 2], a_[4 * (q & 3) + 3]));
          SB();
        });
#pragma unroll
        for (int i = 22 - AV2AT; i < 14; i++) LD_AV2(i);
        SB();
      }
      {
        f16v acc0 = {}, acc1 = {};
        sfor<0, 14>([&](auto II) {
          constexpr int i = decltype(II)::v;
          if constexpr (i % 4 == 0) __builtin_amdgcn_s_setprio(3 - i / 4);
          acc0 = mfma32(wd[2 * i], av2[i], acc0); acc1 = mfma32(wd[2 * i + 1], av2[i], acc1);
          SB();
        });
        if (__builtin_amdgcn_ballot_w64(acc0[0] != acc0[0]) != 0) {
          DBG(11);
#pragma nounroll
          for (int spin = 0; spin < SPIN_CAP; spin++) {
            MEMBAR();
            unsigned mm = 0;
            lo = fresh(l16);
#pragma unroll
            for (int i = 0; i < 14; i++) LD_AV2(i);
#pragma unroll
            for (int i = 0; i < 14; i++) mm = mx4(mm, av2[i]);
            if (!bad(mm)) break;
          }
          acc0 = f16v{}; acc1 = f16v{};
#pragma unroll
          for (int i = 0; i < 14; i++) { acc0 = mfma32(wd[2 * i], av2[i], acc0); acc1 = mfma32(wd[2 * i + 1], av2[i], acc1); }
        }
        STAMP(22);
        const int r0 = fresh(L_H1 + rw[0]), r1 = fresh(L_H1 + rw[1]);
#pragma unroll
        for (int q = 0; q < 4; q++) {
          lds_wr(sm, ((q & 1) ? r1 : r0) + q * 1024, f4u(acc0[4 * q], acc0[4 * q + 1], acc0[4 * q + 2], acc0[4 * q + 3]));
          lds_wr(sm, ((q & 1) ? r1 : r0) + (4 + q) * 1024, f4u(acc1[4 * q], acc1[4 * q + 1], acc1[4 * q + 2], acc1[4 * q + 3]));
        }
        BAR();
        PIN(res);
        __builtin_amdgcn_raw_buffer_store_b128(red_final<16>(sm, fresh((w < 4 ? L_H0 : L_H1) + rr), res), os2, oo, 0, OAUX);
      }
    }
    }
  }
  asm volatile(".Lfb_end:");
  STAMP(23);
}

extern "C" void kernel_launch(void* const* d_in, const int* in_sizes, int n_in, void* d_out, int out_size, void* d_ws, size_t ws_size, hipStream_t stream) {
  Ptrs p;
  p.x = d_in[0]; p.wqkv = d_in[1]; p.wo = d_in[2]; p.wg = d_in[4]; p.wu = d_in[5]; p.wd = d_in[6]; p.out = (char*)d_out; p.ws = (char*)d_ws;
  fused_block<<<256, 512, 0, stream>>>(p);
}
```
